```python
import torch

def _softmax(x, axis=-1):
    return torch.softmax(x, dim=axis)
def _gelu(x):
    return torch.nn.functional.gelu(x, approximate="tanh")
def _silu(x):
    return torch.nn.functional.silu(x)
def _sigmoid(x):
    return torch.sigmoid(x)
def _layernorm(x, axis=-1, eps=1e-5):
    m = x.mean(dim=axis, keepdim=True)
    v = x.var(dim=axis, keepdim=True, unbiased=False)
    return (x - m) / torch.sqrt(v + eps)
def _scatter_add(tgt, idx, src, axis=0):
    return tgt.clone().index_add_(axis, idx, src)
def _segment_sum(src, seg, n):
    out = torch.zeros((n,) + src.shape[1:], dtype=src.dtype)
    return out.index_add_(0, seg, src)
def _pad(x, pw):
    flat = tuple(v for pair in reversed(pw) for v in pair)
    return torch.nn.functional.pad(x, flat)

kernel_name = 'interleave_layers__gqa_block_cached__glu_gated_geglu__decode'

def setup_inputs():

    _w = (lambda a: (lambda t: t.to(torch.bfloat16) if t.is_floating_point() else t)(torch.as_tensor(a)))
    def _A_setup():
        B, Sq, D = 1, 1, 576
        Hq, Hkv, Sk, Dh = 32, 16, 65536, 128
        assert Hq % Hkv == 0, (Hq, Hkv)
        return {
            "x": _w((lambda *s: torch.randn(*s, dtype=torch.bfloat16) * 0.05)(B, Sq, D)),
            "k_cache": _w((lambda *s: torch.randn(*s, dtype=torch.bfloat16) * 0.8)(B, Hkv, Sk, Dh)),
            "v_cache": _w((lambda *s: torch.randn(*s, dtype=torch.bfloat16) * 1.6)(B, Hkv, Sk, Dh)),
            "wq": _w((lambda *s: torch.randn(*s, dtype=torch.bfloat16) * 0.8)(D, Hq * Dh) * (D ** -0.5)),
            "wo": _w((lambda *s: torch.randn(*s, dtype=torch.bfloat16) * 1.6)(Hq * Dh, D) * ((Hq * Dh) ** -0.5)),
        }
    _A_in = _A_setup()

    def _B_setup():
        B, S, Dm, Df = 1, 1, 576, 4608
        return {
            "x": _w((lambda *s: torch.randn(*s, dtype=torch.bfloat16) * 0.05)(B, S, Dm)),
            "w_gate": _w((lambda *s: torch.randn(*s, dtype=torch.bfloat16) * 0.4)(Dm, Df) * (Dm ** -0.5)),
            "w_up": _w((lambda *s: torch.randn(*s, dtype=torch.bfloat16) * 0.4)(Dm, Df) * (Dm ** -0.5)),
            "w_down": _w((lambda *s: torch.randn(*s, dtype=torch.bfloat16) * 0.4)(Df, Dm) * (Df ** -0.5)),
        }
    _B_in = _B_setup()

    return {
        "A_x": _A_in["x"],
        "A_k_cache": _A_in["k_cache"],
        "A_v_cache": _A_in["v_cache"],
        "A_wq": _A_in["wq"],
        "A_wo": _A_in["wo"],
        "B_x": _B_in["x"],
        "B_w_gate": _B_in["w_gate"],
        "B_w_up": _B_in["w_up"],
        "B_w_down": _B_in["w_down"],
    }

def reference(A_x, A_k_cache, A_v_cache, A_wq, A_wo, B_x, B_w_gate, B_w_up, B_w_down):
    def _A_ref(x, k_cache, v_cache, wq, wo):
        B, Sq, D = x.shape
        Hq, Dh = 32, 128
        Hkv = k_cache.shape[1]
        rep = Hq // Hkv
        q = (lambda x, *d: x.permute(*d))((x @ wq).reshape(B, Sq, Hq, Dh), 0, 2, 1, 3)
        k = (lambda x, n, axis: torch.repeat_interleave(x, n, dim=axis))(k_cache, rep, axis=1)
        v = (lambda x, n, axis: torch.repeat_interleave(x, n, dim=axis))(v_cache, rep, axis=1)
        scores = torch.einsum("bhqd,bhkd->bhqk", q, k) * (Dh ** -0.5)
        p = _softmax(scores, axis=-1)
        o = torch.einsum("bhqk,bhkd->bhqd", p, v)
        return (lambda x, *d: x.permute(*d))(o, 0, 2, 1, 3).reshape(B, Sq, Hq * Dh) @ wo

    def _B_ref(x, w_gate, w_up, w_down):
        g = torch.einsum("bsd,df->bsf", x, w_gate)
        u = torch.einsum("bsd,df->bsf", x, w_up)
        h = _gelu(g) * u
        return torch.einsum("bsf,fd->bsd", h, w_down)

    x = A_x
    x = x + _A_ref(_layernorm(x, axis=-1), A_k_cache, A_v_cache, A_wq, A_wo)
    x = x + _B_ref(_layernorm(x, axis=-1), B_w_gate, B_w_up, B_w_down)
    x = x + _A_ref(_layernorm(x, axis=-1), A_k_cache, A_v_cache, A_wq, A_wo)
    x = x + _B_ref(_layernorm(x, axis=-1), B_w_gate, B_w_up, B_w_down)
    return x
```

```cpp
#include <hip/hip_runtime.h>
#include <hip/hip_bf16.h>
#include <stdint.h>
#include <utility>

#pragma clang diagnostic ignored "-Winline-asm"

namespace {

typedef unsigned v4u __attribute__((ext_vector_type(4)));
typedef unsigned v2u __attribute__((ext_vector_type(2)));
typedef float f32x4 __attribute__((ext_vector_type(4)));
typedef float f32x2 __attribute__((ext_vector_type(2)));
typedef __bf16 bf16x2 __attribute__((ext_vector_type(2)));
typedef short v4s __attribute__((ext_vector_type(4)));
#define DI __device__ __forceinline__
#define LAS __attribute__((address_space(3)))
#define ACL "a0", "a1", "a2", "a3", "a4", "a5", "a6", "a7", "a8", "a9", "a10", "a11", "a12", "a13", "a14", "a15", "a16", "a17", "a18", "a19", "a20", "a21", "a22", "a23", "a24", "a25", "a26", "a27", "a28", "a29", "a30", "a31", "a32", "a33", "a34", "a35", "a36", "a37", "a38", "a39", "a40", "a41", "a42", "a43", "a44", "a45", "a46", "a47", "a48", "a49", "a50", "a51", "a52", "a53", "a54", "a55", "a56", "a57", "a58", "a59", "a60", "a61", "a62", "a63"

constexpr int DM = 576, SK = 65536, DF = 4608;
#define XTRA 1
constexpr int NP = 128 - 3 * XTRA;
constexpr int NF = 128 + XTRA;
constexpr int CAP = 64;
constexpr int NVU = (256 - CAP) / 16, NUNITS = NVU + 16;
constexpr int RING = 4, NR = NUNITS - RING;
constexpr int NL = 4, NW = 5;
constexpr int JQ = 12;
constexpr float C2 = 0.088388347648318440f * 1.4426950408889634f;
constexpr unsigned SPIN_CAP = 1u << 17;

constexpr int LDS_WIN = 0, WIN_DN = 55296, WIN_BYTES = 82944;
constexpr int LDS_RET = WIN_BYTES;
constexpr int LDS_MISC = LDS_RET + 4 * NL * 4096;
constexpr int LDS_XH = LDS_MISC;
constexpr int LDS_ZERO = LDS_MISC + 5120;
constexpr int LDS_CMB = LDS_MISC + 5376;
constexpr int LDS_XO = LDS_MISC + 9600;
constexpr int LDS_RED = LDS_MISC + 9856;
constexpr int LDS_BYTES = 163840;
static_assert(LDS_RED + 1024 <= LDS_BYTES && 4 * NW * 4096 <= WIN_BYTES, "LDS map");

constexpr unsigned WS_EPOCH = 0;
constexpr unsigned WS_Q = 4096, Q_BYTES = 1024 * 16;
constexpr unsigned WS_X = WS_Q + 2 * Q_BYTES, X_BYTES = 4096;
constexpr unsigned WS_PART = WS_X + 4 * X_BYTES, PART_H = 80 * 16, PART_BYTES = 256 * 2 * PART_H;
constexpr unsigned WS_YO = WS_PART + 2 * PART_BYTES, YO_BYTES = 36 * 64 * 128;
constexpr unsigned WS_YD = WS_YO + 2 * YO_BYTES, YD_BYTES = 36 * 192 * 128;
constexpr unsigned WS_END = WS_YD + 2 * YD_BYTES;
constexpr unsigned WS_STAMP = (WS_END + 4095u) & ~4095u;

struct Args {
  const unsigned char *x0, *k, *v, *wq, *wo, *wg, *wu, *wd;
  unsigned char *out, *ws;
};

template <int... I, class F> DI void sfor_impl(std::integer_sequence<int, I...>, F&& f) { (f(std::integral_constant<int, I>{}), ...); }
template <int N, class F> DI void sfor(F&& f) { sfor_impl(std::make_integer_sequence<int, N>{}, f); }

template <int CTRL> DI float dppf(float x) { return __builtin_bit_cast(float, __builtin_amdgcn_mov_dpp(__builtin_bit_cast(int, x), CTRL, 0xf, 0xf, true)); }
template <int CTRL> DI unsigned dppu(unsigned x) { return (unsigned)__builtin_amdgcn_mov_dpp((int)x, CTRL, 0xf, 0xf, true); }
constexpr int QP0 = 0x00, QP1 = 0x55, QP2 = 0xAA, QP3 = 0xFF, XOR1 = 0xB1, XOR2 = 0x4E, HMIR = 0x141, ROR4 = 0x124, ROR8 = 0x128, SHR4 = 0x114, SHL4 = 0x104;
DI unsigned cvtpk(float lo, float hi) { f32x2 v = {lo, hi}; return __builtin_bit_cast(unsigned, __builtin_convertvector(v, bf16x2)); }
DI float bflo(unsigned u) { return __builtin_bit_cast(float, u << 16); }
DI float bfhi(unsigned u) { return __builtin_bit_cast(float, u & 0xffff0000u); }
DI float rbf(float x) { return bflo(cvtpk(x, x)); }
DI float xrow_max(float x) {
  auto s = __builtin_amdgcn_permlane16_swap(__float_as_uint(x), __float_as_uint(x), false, false);
  x = fmaxf(__uint_as_float(s[0]), __uint_as_float(s[1]));
  auto t = __builtin_amdgcn_permlane32_swap(__float_as_uint(x), __float_as_uint(x), false, false);
  return fmaxf(__uint_as_float(t[0]), __uint_as_float(t[1]));
}
DI float xrow_sum(float x) {
  auto s = __builtin_amdgcn_permlane16_swap(__float_as_uint(x), __float_as_uint(x), false, false);
  x = __uint_as_float(s[0]) + __uint_as_float(s[1]);
  auto t = __builtin_amdgcn_permlane32_swap(__float_as_uint(x), __float_as_uint(x), false, false);
  return __uint_as_float(t[0]) + __uint_as_float(t[1]);
}
DI float wave_sum(float x) {
  x += dppf<XOR1>(x); x += dppf<XOR2>(x); x += dppf<ROR4>(x); x += dppf<ROR8>(x);
  return xrow_sum(x);
}
DI void wg_barrier() { asm volatile("s_waitcnt lgkmcnt(0)\n\ts_barrier" ::: "memory", ACL); }
template <int N> DI void vmwait() { asm volatile("s_waitcnt vmcnt(%c0)" ::"i"(N) : "memory", ACL); }
DI void lgkwait() { asm volatile("s_waitcnt lgkmcnt(0)" ::: "memory", ACL); }

#define LDSP(T, off) ((LAS T*)(uintptr_t)(off))
DI v4u lds_r128(unsigned a) { return *LDSP(v4u, a); }
DI v2u lds_tr(unsigned a) { return __builtin_bit_cast(v2u, __builtin_amdgcn_ds_read_tr16_b64_v4i16(LDSP(v4s, a))); }
DI v4u lds_frag(unsigned a, unsigned second) { const v2u lo = lds_tr(a), hi = lds_tr(a + second); return v4u{lo.x, lo.y, hi.x, hi.y}; }

typedef __amdgpu_buffer_rsrc_t rsrc_t;
DI rsrc_t mk_rsrc(const unsigned char* p, unsigned bytes) { return __builtin_amdgcn_make_buffer_rsrc((void*)p, 0, bytes, 0x00020000); }
DI int opq(int x) { asm volatile("" : "+v"(x)::ACL); return x; }
DI unsigned opqs(unsigned x) { asm volatile("" : "+s"(x)); return x; }

DI void glds_b(unsigned voff, rsrc_t r, unsigned soff, unsigned lds_dst) {
  unsigned keep;
  asm volatile("s_mov_b32 %0, m0\n\ts_mov_b32 m0, %4\n\ts_nop 0\n\tbuffer_load_dwordx4 %1, %2, %3 offen nt lds\n\ts_mov_b32 m0, %0"
               : "=&s"(keep) : "v"(voff), "s"(r), "s"(soff), "s"(lds_dst) : "memory");
}
DI void glds_v48(const unsigned char* vaddr, unsigned lds_dst) {
  unsigned keep;
  asm volatile("s_mov_b32 %0, m0\n\ts_mov_b32 m0, %2\n\ts_mov_b32 exec_hi, 0xffff\n\tglobal_load_lds_dwordx4 %1, off nt\n\ts_mov_b32 exec_hi, -1\n\ts_mov_b32 m0, %0"
               : "=&s"(keep) : "v"(vaddr), "s"(lds_dst) : "memory");
}
struct KV { rsrc_t k, v; unsigned koff, voff, so; };
DI void glds_unit(KV& s, unsigned lds_dst) {
  unsigned keep;
  asm volatile("s_mov_b32 %0, m0\n\ts_mov_b32 m0, %6\n\ts_nop 0\n\t"
               "buffer_load_dwordx4 %2, %3, %1 offen nt lds\n\tbuffer_load_dwordx4 %2, %3, %1 offen offset:1024 nt lds\n\t"
               "s_add_u32 m0, m0, 2048\n\ts_nop 0\n\t"
               "buffer_load_dwordx4 %4, %5, %1 offen nt lds\n\tbuffer_load_dwordx4 %4, %5, %1 offen offset:1024 nt lds\n\ts_mov_b32 m0, %0\n\ts_add_u32 %1, %1, 8192"
               : "=&s"(keep), "+s"(s.so) : "v"(s.koff), "s"(s.k), "v"(s.voff), "s"(s.v), "s"(lds_dst) : "memory", "scc");
}

template <int U> DI void unit_load(KV& s) {
  constexpr int B = U < NVU ? CAP + 16 * U : 16 * (U - NVU);
#define UL(R) asm volatile("buffer_load_dwordx4 " R "[%c5:%c6], %1, %2, %0 offen nt\n\tbuffer_load_dwordx4 " R "[%c7:%c8], %1, %2, %0 offen offset:1024 nt\n\t" \
                           "buffer_load_dwordx4 " R "[%c9:%c10], %3, %4, %0 offen nt\n\tbuffer_load_dwordx4 " R "[%c11:%c12], %3, %4, %0 offen offset:1024 nt\n\t" \
                           "s_add_u32 %0, %0, 8192" \
                           : "+s"(s.so) : "v"(s.koff), "s"(s.k), "v"(s.voff), "s"(s.v), "i"(B), "i"(B + 3), "i"(B + 4), "i"(B + 7), "i"(B + 8), "i"(B + 11), "i"(B + 12), "i"(B + 15) : "memory", "scc")
  if constexpr (U < NVU) UL("v"); else UL("a");
#undef UL
}
template <int U> DI void unit_to_lds(unsigned a) {
  constexpr int B = U < NVU ? CAP + 16 * U : 16 * (U - NVU);
#define UW(R) asm volatile("ds_write_b128 %0, " R "[%c1:%c2]\n\tds_write_b128 %0, " R "[%c3:%c4] offset:1024\n\t" \
                           "ds_write_b128 %0, " R "[%c5:%c6] offset:2048\n\tds_write_b128 %0, " R "[%c7:%c8] offset:3072\n\ts_waitcnt lgkmcnt(0)" \
                           ::"v"(a), "i"(B), "i"(B + 3), "i"(B + 4), "i"(B + 7), "i"(B + 8), "i"(B + 11), "i"(B + 12), "i"(B + 15) : "memory")
  if constexpr (U < NVU) UW("v"); else UW("a");
#undef UW
}
template <int U> DI void unit_from_lds(unsigned a) {
  constexpr int B = U < NVU ? CAP + 16 * U : 16 * (U - NVU);
#define UR(R) asm volatile("ds_read_b128 " R "[%c1:%c2], %0\n\tds_read_b128 " R "[%c3:%c4], %0 offset:1024\n\t" \
                           "ds_read_b128 " R "[%c5:%c6], %0 offset:2048\n\tds_read_b128 " R "[%c7:%c8], %0 offset:3072" \
                           ::"v"(a), "i"(B), "i"(B + 3), "i"(B + 4), "i"(B + 7), "i"(B + 8), "i"(B + 11), "i"(B + 12), "i"(B + 15) : "memory")
  if constexpr (U < NVU) UR("v"); else UR("a");
#undef UR
}

struct Att { v4u q0, q1; unsigned msk[4]; f32x4 acc; float m, l; };

template <int U> DI void unit_proc(Att& s) {
  constexpr int B = U < NVU ? CAP + 16 * U : 16 * (U - NVU);
  f32x4 d;
#define QK(R) asm volatile("s_nop 1\n\tv_mfma_f32_16x16x32_bf16 %0, " R "[%c3:%c4], %1, 0\n\tv_mfma_f32_16x16x32_bf16 %0, " R "[%c5:%c6], %2, %0\n\ts_nop 7" \
                           : "=&v"(d) : "v"(s.q0), "v"(s.q1), "i"(B), "i"(B + 3), "i"(B + 4), "i"(B + 7) : "memory", ACL)
  if constexpr (U < NVU) QK("v"); else QK("a");
#undef QK
  const float sc = ((dppf<QP0>(d[0]) + dppf<QP1>(d[1])) + (dppf<QP2>(d[2]) + dppf<QP3>(d[3]))) * C2;
  const float mx = xrow_max(fmaxf(sc, dppf<HMIR>(sc)));
  const float mn = fmaxf(s.m, mx);
  const float al = __builtin_amdgcn_exp2f(s.m - mn);
  const float p = __builtin_amdgcn_exp2f(sc - mn);
  s.m = mn; s.l = s.l * al + p;
  s.acc *= al;
  const unsigned pp = cvtpk(p, p), pl = dppu<SHR4>(pp), pr = dppu<SHL4>(pp);
  const v4u p0 = {pp & s.msk[0], pp & s.msk[1], pl & s.msk[2], pl & s.msk[3]};
  const v4u p1 = {pr & s.msk[0], pr & s.msk[1], pp & s.msk[2], pp & s.msk[3]};
#define PV(R) asm volatile("s_nop 1\n\tv_mfma_f32_16x16x32_bf16 %0, " R "[%c4:%c5], %2, %0\n\tv_mfma_f32_16x16x32_bf16 %0, " R "[%c6:%c7], %3, %0\n\ts_nop 7" \
                           : "+v"(s.acc), "+v"(s.l) : "v"(p0), "v"(p1), "i"(B + 8), "i"(B + 11), "i"(B + 12), "i"(B + 15) : "memory", ACL)
  if constexpr (U < NVU) PV("v"); else PV("a");
#undef PV
}

DI void mfma1(f32x4& d, v4u a, v4u b) { asm volatile("s_nop 1\n\tv_mfma_f32_16x16x32_bf16 %0, %1, %2, %0" : "+v"(d) : "v"(a), "v"(b)); }
DI void mfma2(f32x4& d, v4u a0, v4u b0, v4u a1, v4u b1) {
  asm volatile("s_nop 1\n\tv_mfma_f32_16x16x32_bf16 %0, %1, %2, %0\n\tv_mfma_f32_16x16x32_bf16 %0, %3, %4, %0" : "+v"(d) : "v"(a0), "v"(b0), "v"(a1), "v"(b1));
}
DI void mfma3(f32x4& d0, f32x4& d1, f32x4& d2, v4u a, v4u b0, v4u b1, v4u b2) {
  asm volatile("s_nop 1\n\tv_mfma_f32_16x16x32_bf16 %0, %3, %4, %0\n\tv_mfma_f32_16x16x32_bf16 %1, %3, %5, %1\n\tv_mfma_f32_16x16x32_bf16 %2, %3, %6, %2"
               : "+v"(d0), "+v"(d1), "+v"(d2) : "v"(a), "v"(b0), "v"(b1), "v"(b2));
}
DI void mfma_fence(f32x4& d) { asm volatile("s_nop 7" : "+v"(d)::ACL); }

#define STAMP(i) do {} while (0)

__global__ void __launch_bounds__(256, 1) __attribute__((amdgpu_num_vgpr(CAP)))
fused_decode_kernel(Args a) {
  extern __shared__ __attribute__((aligned(16))) unsigned char lds_image[];
  asm volatile("" ::"v"((unsigned)(uintptr_t)(LAS unsigned char*)lds_image) : "v255", "a255");
  const int tid0 = threadIdx.x;
  const unsigned w0 = __builtin_amdgcn_readfirstlane(tid0 >> 6);
  const bool isP = ((blockIdx.x >> 3) & 3) == 0;
  const bool isR = isP && (blockIdx.x >> 5) * 8 + (blockIdx.x & 7) < 36;
  const int N = isP ? NP : NF;
#define LANES const int tid = opq(tid0), lane = tid & 63, g4 = lane >> 4, c16 = lane & 15; const int w = opqs(w0), bx = opqs(blockIdx.x); \
  const int xcd = bx & 7, bi = bx >> 3, kvh = 2 * xcd + (bi >> 4), sp = bi & 15, pidx = 4 * kvh + (sp >> 2), \
            fidx = 24 * xcd + (bi >> 4) * 12 + (sp - (sp >> 2) - 1), ridx = (bi >> 2) * 8 + xcd; \
  (void)g4; (void)c16; (void)w; (void)pidx; (void)fidx; (void)ridx

  const rsrc_t rs = mk_rsrc(a.ws, WS_END);
  KV kv;
  {
    LANES;
    const int nP_before = (sp + 3) >> 2;
    const int row0 = nP_before * NP + (sp - nP_before) * NF;
    kv.k = mk_rsrc(a.k + ((size_t)kvh * SK + (size_t)row0 * 32) * 256, N * 8192);
    kv.v = mk_rsrc(a.v + ((size_t)kvh * SK + (size_t)row0 * 32) * 256, N * 8192);
    kv.koff = w * 2048 + (c16 >> 2) * 256 + g4 * 64 + (c16 & 3) * 16;
    kv.voff = w * 2048 + lane * 16;
    kv.so = 0;
  }
  STAMP(0);

  auto load_wq = [&]() {
    LANES;
    const rsrc_t r = mk_rsrc(a.wq + pidx * 128, DM * 8192 - pidx * 128);
    const unsigned o = (lane >> 3) * 8192 + (((lane & 7) ^ (((lane >> 4) & 3) << 1)) << 4);
#pragma unroll 1
    for (int pc = w; pc < 72; pc += 4) glds_b(o, r, pc * 65536, LDS_WIN + pc * 1024);
  };
  auto load_rows = [&](bool down) {
    LANES;
    const int npieces = down ? 27 : 72;
    const unsigned dst = down ? LDS_WIN + WIN_DN : LDS_WIN;
    const rsrc_t r = mk_rsrc(down ? a.wd + (size_t)fidx * 24 * 1152 : a.wo + (size_t)pidx * 64 * 1152, npieces * 1024);
#pragma unroll 1
    for (int pc = w; pc < npieces; pc += 4) {
      const unsigned idx = pc * 64 + lane, row = idx / 72u, cc = idx - row * 72u;
      glds_b(row * 1152 + ((cc ^ (((row >> 1) & 3) << 1)) << 4), r, 0, dst + pc * 1024);
    }
  };
  auto load_ffn = [&]() {
    LANES;
    const int l48 = lane < 48 ? lane : 0, r = l48 / 6, cc = l48 - r * 6;
    const unsigned char* src = (cc < 3 ? a.wg + cc * 16 : a.wu + (cc - 3) * 16) + fidx * 48 + (size_t)(8 * w + r) * (DF * 2);
#pragma unroll 1
    for (int pc = w; pc < 72; pc += 4) { glds_v48(src, LDS_WIN + pc * 768); src += 32 * DF * 2; }
    load_rows(true);
  };
  auto refill_window = [&]() {
    const int w = opqs(w0);
    kv.so = opqs((NR + NL) * 8192);
#pragma unroll 1
    for (int i = 0; i < NW; ++i) glds_unit(kv, LDS_WIN + (w * NW + i) * 4096);
  };

  const unsigned epoch_v = __builtin_amdgcn_raw_buffer_load_b32(rs, WS_EPOCH, 0, 16);
  unsigned xres = 0;
  v2u xr[3] = {{0, 0}, {0, 0}, {0, 0}};
  {
    LANES;
    if (isR) xres = *(const unsigned short*)(a.x0 + (16 * ridx + c16) * 2);
    if (tid < 16) *LDSP(unsigned, LDS_ZERO + tid * 4) = 0u;
    if (isP) {
#pragma unroll
      for (int i = 0; i < 3; ++i) xr[i] = *(const v2u*)(a.x0 + ((i < 2 || lane < 16) ? (lane + 64 * i) * 8 : 0));
      load_wq();
    }
  }
  sfor<3>([&](auto I) { unit_load<RING + I.value>(kv); });

  const unsigned ep = __builtin_amdgcn_readfirstlane(epoch_v);
  const unsigned tagA = ep + 1u, tagB = (ep + 1u) * 0x9E3779B1u + 0x7F4A7C15u;
  bool dead = false;
  auto ok4 = [&](const v4u& v) { return v.y == tagA && v.w == tagB; };
  auto put = [&](unsigned off, unsigned v0, unsigned v1) { __builtin_amdgcn_raw_buffer_store_b128(v4u{v0, tagA, v1, tagB}, rs, off, 0, 16); };
  auto get = [&](unsigned off) { return __builtin_amdgcn_raw_buffer_load_b128(rs, off, 0, 16); };

  auto layer_norm = [&]() {
    LANES;
    float x[12];
#pragma unroll
    for (int i = 0; i < 3; ++i) { x[4 * i] = bflo(xr[i].x); x[4 * i + 1] = bfhi(xr[i].x); x[4 * i + 2] = bflo(xr[i].y); x[4 * i + 3] = bfhi(xr[i].y); }
    const float c0 = __builtin_bit_cast(float, __builtin_amdgcn_readfirstlane(__builtin_bit_cast(int, x[0])));
    float s1 = 0.f, s2 = 0.f;
#pragma unroll
    for (int i = 0; i < 12; ++i) { const float d = (i < 8 || lane < 16) ? x[i] - c0 : 0.f; s1 += d; s2 = fmaf(d, d, s2); }
    s1 = wave_sum(s1); s2 = wave_sum(s2);
    const float md = s1 * (1.f / DM);
    const float mean = rbf(c0 + md);
    const float var = rbf(fmaxf(s2 * (1.f / DM) - md * md, 0.f));
    const float den = rbf(__builtin_amdgcn_sqrtf(rbf(var + 1e-5f)));
    const float inv = 1.f / den;
#pragma unroll
    for (int i = 0; i < 3; ++i) {
      const unsigned d0 = cvtpk(x[4 * i] - mean, x[4 * i + 1] - mean), d1 = cvtpk(x[4 * i + 2] - mean, x[4 * i + 3] - mean);
      const v2u y = {cvtpk(bflo(d0) * inv, bfhi(d0) * inv), cvtpk(bflo(d1) * inv, bfhi(d1) * inv)};
      const int slot = lane + 64 * i;
      if (i < 2 || lane < 16) *LDSP(v2u, LDS_XH + w * 1280 + ((slot & ~7) + (slot & 3) * 2 + ((slot >> 2) & 1)) * 8) = y;
    }
    lgkwait();
  };
  auto gather_x = [&](unsigned base) {
    LANES;
    for (unsigned spin = 0;; ++spin) {
      asm volatile("" ::: "memory");
      v4u t[3];
#pragma unroll
      for (int i = 0; i < 3; ++i) t[i] = get(base + ((i < 2 || lane < 16) ? (lane + 64 * i) : lane) * 16);
#pragma unroll
      for (int i = 0; i < 3; ++i) xr[i] = v2u{t[i].x, t[i].z};
      if (__all(ok4(t[0]) && ok4(t[1]) && ok4(t[2])) || dead) break;
      if (spin > SPIN_CAP) { dead = true; break; }
    }
  };
  auto proj_rows = [&](auto KSc, unsigned ybase) {
    constexpr int KS = KSc.value;
    LANES;
    const unsigned img = KS == 2 ? LDS_WIN : LDS_WIN + WIN_DN, xsrc = KS == 2 ? LDS_XO : LDS_XO + w * 64, nprod = KS == 2 ? 64 : 192, prod = KS == 2 ? pidx : fidx;
    const unsigned hl = (2 * g4 + (c16 >> 3)) & 3, trow = 4 * g4 + (c16 >> 2);
    const unsigned wa = img + trow * 1152 + (((unsigned)w ^ hl) << 5) + (lane & 3) * 8;
    const unsigned second = (KS == 1 && g4 >= 2) ? 8 * 1152 : 16 * 1152;
    f32x4 d[3] = {{0, 0, 0, 0}, {0, 0, 0, 0}, {0, 0, 0, 0}};
#pragma unroll
    for (int G = 0; G < 4; ++G) {
      const bool act = (c16 >> 2) == G;
      const v4u a0 = lds_r128(act ? xsrc + g4 * 16 : LDS_ZERO);
      if constexpr (KS == 2) {
        const v4u a1 = lds_r128(act ? xsrc + 64 + g4 * 16 : LDS_ZERO);
#pragma unroll
        for (int u = 0; u < 3; ++u) if (4 * u + G < 9) mfma2(d[u], a0, lds_frag(wa + (4 * u + G) * 128, second), a1, lds_frag(wa + (4 * u + G) * 128 + 32 * 1152, second));
      } else {
#pragma unroll
        for (int u = 0; u < 3; ++u) if (4 * u + G < 9) mfma1(d[u], a0, lds_frag(wa + (4 * u + G) * 128, second));
      }
    }
#pragma unroll
    for (int u = 0; u < 3; ++u) {
      mfma_fence(d[u]);
      const float nb = dppf<XOR1>(d[u][0]);
      const unsigned T = w + 4 * (4 * u + g4);
      if (!(lane & 1) && (u < 2 || g4 == 0)) put(ybase + (T * nprod + prod) * 128 + (c16 >> 1) * 16, __float_as_uint(d[u][0]), __float_as_uint(nb));
    }
  };
  auto reduce_cols = [&](auto NIc, unsigned ybase) {
    constexpr int NI = NIc.value;
    LANES;
    float s0 = 0.f, s1 = 0.f;
    for (unsigned spin = 0;; ++spin) {
      asm volatile("" ::: "memory");
      v4u t[NI];
#pragma unroll
      for (int i = 0; i < NI; ++i) t[i] = get(ybase + ridx * (NI * 4096) + i * 4096 + tid * 16);
      bool ok = true; s0 = 0.f; s1 = 0.f;
#pragma unroll
      for (int i = 0; i < NI; ++i) { ok = ok && ok4(t[i]); s0 += __uint_as_float(t[i].x); s1 += __uint_as_float(t[i].z); }
      if (__all(ok) || dead) break;
      if (spin > SPIN_CAP) { dead = true; break; }
    }
    s0 += dppf<ROR8>(s0); s1 += dppf<ROR8>(s1);
    s0 = xrow_sum(s0); s1 = xrow_sum(s1);
    if (lane < 8) *LDSP(f32x2, LDS_RED + w * 64 + lane * 8) = f32x2{s0, s1};
    wg_barrier();
    const float y = (*LDSP(float, LDS_RED + c16 * 4) + *LDSP(float, LDS_RED + 64 + c16 * 4)) + (*LDSP(float, LDS_RED + 128 + c16 * 4) + *LDSP(float, LDS_RED + 192 + c16 * 4));
    xres = cvtpk(bflo(xres) + rbf(y), 0.f) & 0xffffu;
    return v2u{xres | (dppu<QP1>(xres) << 16), dppu<QP2>(xres) | (dppu<QP3>(xres) << 16)};
  };

#pragma unroll 1
  for (int it = 0; it < 2; ++it) {
    if (isP) {
      if (it == 1) gather_x(WS_X + 1 * X_BYTES);
      layer_norm();
      if (it == 0) vmwait<12>(); else vmwait<0>();
      wg_barrier();
      STAMP(1 + 16 * it);
      LANES;
      const unsigned hl = (2 * g4 + (c16 >> 3)) & 3, trow = 4 * g4 + (c16 >> 2);
      const unsigned wa = LDS_WIN + trow * 128 + (((unsigned)w ^ hl) << 5) + (lane & 3) * 8;
      f32x4 d0 = {0, 0, 0, 0}, d1 = {0, 0, 0, 0};
#pragma unroll
      for (int ks = 0; ks < 18; ks += 2) {
        mfma1(d0, lds_r128(LDS_XH + w * 1280 + ks * 64 + g4 * 16), lds_frag(wa + ks * 4096, 2048));
        mfma1(d1, lds_r128(LDS_XH + w * 1280 + ks * 64 + 64 + g4 * 16), lds_frag(wa + ks * 4096 + 4096, 2048));
      }
      mfma_fence(d0); mfma_fence(d1);
      const unsigned qb = cvtpk(d0[0] + d1[0], 0.f) & 0xffffu;
      const unsigned v0 = qb | (dppu<QP1>(qb) << 16), v1 = dppu<QP2>(qb) | (dppu<QP3>(qb) << 16);
      if (lane < 16 && !(lane & 3)) put(WS_Q + it * Q_BYTES + (pidx * 16 + w * 4 + (lane >> 2)) * 16, v0, v1);
      STAMP(2 + 16 * it);
      if (it == 1) { wg_barrier(); refill_window(); }
    }

    Att s;
    unsigned qoff, lret, lwin;
    {
      LANES;
#pragma unroll
      for (int i = 0; i < 4; ++i) s.msk[i] = ((c16 & 7) >> 1) == i ? ((c16 & 1) ? 0xffff0000u : 0xffffu) : 0u;
      qoff = WS_Q + it * Q_BYTES + ((2 * kvh + (c16 >> 3)) * 32 + 2 * (4 * g4 + (c16 & 3))) * 16;
      lret = LDS_RET + w * (NL * 4096) + lane * 16; lwin = LDS_WIN + w * (NW * 4096) + lane * 16;
    }
    auto q_issue = [&](v4u& t0, v4u& t1) {
      asm volatile("buffer_load_dwordx4 %0, %2, %3, 0 offen sc1\n\tbuffer_load_dwordx4 %1, %2, %3, 0 offen offset:16 sc1" : "=&v"(t0), "=&v"(t1) : "v"(qoff), "s"(rs) : "memory");
    };
    auto q_finish = [&](v4u& t0, v4u& t1) {
      for (unsigned spin = 0; !(__all(ok4(t0) && ok4(t1)) || dead); ++spin) {
        if (spin > SPIN_CAP) { dead = true; break; }
        q_issue(t0, t1);
        asm volatile("s_waitcnt vmcnt(0)" : "+v"(t0), "+v"(t1)::"memory");
      }
      const v4u q = {t0.x, t0.z, t1.x, t1.z}, z = {0, 0, 0, 0};
      const bool hi = (opq(tid0) & 4) != 0;
      s.q0 = hi ? z : q; s.q1 = hi ? q : z;
      s.acc = f32x4{0, 0, 0, 0}; s.m = -3.0e38f; s.l = 0.f;
    };
    if (it == 0) {
      sfor<JQ - 3>([&](auto I) { vmwait<8>(); unit_load<RING + 3 + I.value>(kv); });
      v4u t0, t1;
      vmwait<8>();
      q_issue(t0, t1);
      unit_load<RING + JQ>(kv);
      asm volatile("s_waitcnt vmcnt(4)" : "+v"(t0), "+v"(t1)::"memory");
      q_finish(t0, t1);
      STAMP(3);
      constexpr int REM = (NP - NR - NL) & 3, JS = NR + NL + REM + 4;
      static_assert(2 * (NR + 3 - JQ - 1) >= NR, "backlog must be gone before the ring wraps");
      sfor<JS - JQ - 1>([&](auto I) {
        constexpr int j = JQ + 1 + I.value;
        vmwait<8>();
        if constexpr (j < NR) unit_load<RING + j>(kv); else unit_load<(j - NR) & 3>(kv);
        constexpr int done = (2 * I.value < j - 3) ? 2 * I.value : j - 3;
        constexpr int upto = (2 * I.value + 2 < j - 2) ? 2 * I.value + 2 : j - 2;
        sfor<upto - done>([&](auto K) {
          constexpr int u = done + K.value;
          if constexpr (u < NR) unit_proc<RING + u>(s);
          else { unit_proc<(u - NR) & 3>(s); if constexpr (u < NR + NL) unit_to_lds<(u - NR) & 3>(lret + (u - NR) * 4096); }
        });
      });
#pragma unroll 1
      for (int j = JS; j < N; j += 4) {
        sfor<4>([&](auto I) {
          vmwait<8>();
          unit_load<(JS + I.value - NR) & 3>(kv);
          unit_proc<(JS + I.value - 3 - NR) & 3>(s);
        });
      }
      if (isP) {
        load_rows(false);
        vmwait<26>(); unit_proc<(JS - 3 - NR) & 3>(s);
        vmwait<22>(); unit_proc<(JS - 2 - NR) & 3>(s);
        vmwait<18>(); unit_proc<(JS - 1 - NR) & 3>(s);
      } else {
        vmwait<8>(); unit_proc<(JS - 3 - NR) & 3>(s);
        vmwait<4>(); unit_proc<(JS - 2 - NR) & 3>(s);
        vmwait<0>(); unit_proc<(JS - 1 - NR) & 3>(s);
      }
    } else {
      v4u t0, t1;
      q_issue(t0, t1);
      asm volatile("s_waitcnt vmcnt(0)" : "+v"(t0), "+v"(t1)::"memory");
      q_finish(t0, t1);
      STAMP(19);
      constexpr int NON = NR + NL + NW;
      constexpr int T1 = 3 + NON + ((NP - RING - NON - 3 - NON) & 3);
      sfor<4>([&](auto I) {
        unit_proc<I.value>(s);
        if constexpr (I.value < 3) unit_load<I.value>(kv);
      });
      sfor<T1 - 3>([&](auto I) {
        constexpr int t = 3 + I.value, o = I.value;
        if constexpr (o >= NR && o < NR + NL) unit_from_lds<RING + (o & 1)>(lret + (o - NR) * 4096);
        if constexpr (o < NR) unit_proc<RING + o>(s);
        vmwait<8>();
        unit_load<t & 3>(kv);
        if constexpr (o >= NR + NL && o < NON) unit_from_lds<RING + (o & 1)>(lwin + (o - NR - NL) * 4096);
        unit_proc<(t - 3) & 3>(s);
        if constexpr (o >= NR && o < NON) { lgkwait(); unit_proc<RING + (o & 1)>(s); }
      });
#pragma unroll 1
      for (int t = T1; t < N - RING - NON; t += 4) {
        sfor<4>([&](auto I) {
          vmwait<8>();
          unit_load<(T1 + I.value) & 3>(kv);
          unit_proc<(T1 + I.value - 3) & 3>(s);
        });
      }
      if (isP) {
        wg_barrier();
        load_rows(false);
        vmwait<26>(); unit_proc<(T1 - 3) & 3>(s);
        vmwait<22>(); unit_proc<(T1 - 2) & 3>(s);
        vmwait<18>(); unit_proc<(T1 - 1) & 3>(s);
      } else {
        vmwait<8>(); unit_proc<(T1 - 3) & 3>(s);
        vmwait<4>(); unit_proc<(T1 - 2) & 3>(s);
        vmwait<0>(); unit_proc<(T1 - 1) & 3>(s);
      }
    }
    STAMP(4 + 16 * it);

    {
      LANES;
      float l = s.l + dppf<HMIR>(s.l);
      l = xrow_sum(l);
#pragma unroll
      for (int i = 0; i < 4; ++i) *LDSP(float, LDS_CMB + w * 1024 + i * 256 + lane * 4) = s.acc[i];
      if (lane == 0 || lane == 8) *LDSP(f32x2, LDS_CMB + 4096 + w * 16 + lane) = f32x2{s.m, l};
      wg_barrier();
      if (tid < 128) {
        const int hh = tid >> 6, e = tid & 63;
        f32x2 ml[4];
#pragma unroll
        for (int i = 0; i < 4; ++i) ml[i] = *LDSP(f32x2, LDS_CMB + 4096 + i * 16 + hh * 8);
        const float M = fmaxf(fmaxf(ml[0].x, ml[1].x), fmaxf(ml[2].x, ml[3].x));
        float o0 = 0.f, o1 = 0.f, L = 0.f;
#pragma unroll
        for (int i = 0; i < 4; ++i) {
          const float f = __builtin_amdgcn_exp2f(ml[i].x - M);
          const f32x2 o = *LDSP(f32x2, LDS_CMB + i * 1024 + ((e >> 2) & 3) * 256 + (16 * (e >> 4) + 8 * hh + 2 * (e & 3)) * 4);
          o0 = fmaf(f, o.x, o0); o1 = fmaf(f, o.y, o1); L = fmaf(f, ml[i].y, L);
        }
        const unsigned pb = WS_PART + it * PART_BYTES + ((kvh * 16 + sp) * 2 + hh) * PART_H;
        put(pb + e * 16, __float_as_uint(o0), __float_as_uint(o1));
        if (e == 0) put(pb + 64 * 16, __float_as_uint(M), __float_as_uint(L));
      }
    }
    STAMP(5 + 16 * it);

    if (isP) {
      {
        LANES;
        const int qd = sp >> 2;
        const unsigned pb = WS_PART + it * PART_BYTES + ((kvh * 16 + (lane >> 2)) * 2 + (qd >> 1)) * PART_H;
        v4u t0, t1, tm;
        for (unsigned spin = 0;; ++spin) {
      asm volatile("" ::: "memory");
          t0 = get(pb + ((qd & 1) * 32 + w * 8 + (lane & 3) * 2) * 16);
          t1 = get(pb + ((qd & 1) * 32 + w * 8 + (lane & 3) * 2 + 1) * 16);
          tm = get(pb + 64 * 16);
          if (__all(ok4(t0) && ok4(t1) && ok4(tm)) || dead) break;
          if (spin > SPIN_CAP) { dead = true; break; }
        }
        STAMP(6 + 16 * it);
        const float ms = __uint_as_float(tm.x);
        float M = fmaxf(ms, dppf<ROR4>(ms)); M = fmaxf(M, dppf<ROR8>(M)); M = xrow_max(M);
        const float f = __builtin_amdgcn_exp2f(ms - M);
        float v[5] = {f * __uint_as_float(t0.x), f * __uint_as_float(t0.z), f * __uint_as_float(t1.x), f * __uint_as_float(t1.z), f * __uint_as_float(tm.z)};
#pragma unroll
        for (int i = 0; i < 5; ++i) { v[i] += dppf<ROR4>(v[i]); v[i] += dppf<ROR8>(v[i]); v[i] = xrow_sum(v[i]); }
        const float inv = 1.f / v[4];
        const int slot = 4 * w + lane;
        if (lane < 4) *LDSP(v2u, LDS_XO + ((slot & ~7) + (slot & 3) * 2 + ((slot >> 2) & 1)) * 8) = v2u{cvtpk(v[0] * inv, v[1] * inv), cvtpk(v[2] * inv, v[3] * inv)};
      }
      vmwait<0>();
      wg_barrier();
      proj_rows(std::integral_constant<int, 2>{}, WS_YO + it * YO_BYTES);
      STAMP(7 + 16 * it);
      if (it == 0) { wg_barrier(); load_wq(); }
      if (isR) {
        const v2u xo = reduce_cols(std::integral_constant<int, 2>{}, WS_YO + it * YO_BYTES);
        LANES;
        if (tid < 16 && !(tid & 3)) put(WS_X + (2 * it) * X_BYTES + (ridx * 4 + (tid >> 2)) * 16, xo.x, xo.y);
        STAMP(8 + 16 * it);
        wg_barrier();
        const v2u xn = reduce_cols(std::integral_constant<int, 6>{}, WS_YD + it * YD_BYTES);
        if (tid < 16 && !(tid & 3)) {
          if (it == 0) put(WS_X + X_BYTES + (ridx * 4 + (tid >> 2)) * 16, xn.x, xn.y);
          else *(v2u*)(a.out + (ridx * 16 + tid) * 2) = xn;
        }
        if (it == 1 && ridx == 0 && tid == 0) __builtin_amdgcn_raw_buffer_store_b32(opqs(tagA), rs, WS_EPOCH, 0, 16);
        STAMP(12 + 16 * it);
      }
    } else {
      load_ffn();
      gather_x(WS_X + (2 * it) * X_BYTES);
      STAMP(9 + 16 * it);
      layer_norm();
      vmwait<0>();
      wg_barrier();
      {
        LANES;
        const unsigned wa = LDS_WIN + (32 * w + 4 * g4 + (c16 >> 2)) * 96 + (lane & 3) * 8;
        f32x4 d0 = {0, 0, 0, 0}, d1 = {0, 0, 0, 0}, d2 = {0, 0, 0, 0};
#pragma unroll
        for (int i = 0; i < 5; ++i) {
          if (i < 4 || w < 2) {
            const unsigned b = wa + i * (128 * 96);
            mfma3(d0, d1, d2, lds_r128(LDS_XH + w * 1280 + (w + 4 * i) * 64 + g4 * 16), lds_frag(b, 16 * 96), lds_frag(b + 32, 16 * 96), lds_frag(b + 64, 16 * 96));
          }
        }
        mfma_fence(d0); mfma_fence(d1); mfma_fence(d2);
        if (lane < 48) *LDSP(float, LDS_RED + w * 192 + lane * 4) = g4 == 0 ? d0[0] : (g4 == 1 ? d1[0] : d2[0]);
        wg_barrier();
        const int j = lane < 24 ? lane : 0;
        float gs = 0.f, us = 0.f;
#pragma unroll
        for (int i = 0; i < 4; ++i) { gs += *LDSP(float, LDS_RED + i * 192 + j * 4); us += *LDSP(float, LDS_RED + i * 192 + 96 + j * 4); }
        const float gb = rbf(gs), ub = rbf(us);
        const float z2 = gb * (1.0f + 0.044715f * gb * gb) * (2.0f * 0.7978845608028654f * 1.4426950408889634f);
        const float th = 1.0f - 2.0f / (__builtin_amdgcn_exp2f(z2) + 1.0f);
        const float ge = rbf(0.5f * gb * (1.0f + th));
        const unsigned hb = lane < 24 ? (cvtpk(ge * ub, 0.f) & 0xffffu) : 0u;
        const int q = lane >> 2;
        if (lane < 32) *LDSP(unsigned short, LDS_XO + w * 64 + (((q & 3) * 2 + (q >> 2)) * 4 + (lane & 3)) * 2) = (unsigned short)hb;
        lgkwait();
      }
      STAMP(10 + 16 * it);
      proj_rows(std::integral_constant<int, 1>{}, WS_YD + it * YD_BYTES);
      STAMP(11 + 16 * it);
      if (it == 0) { wg_barrier(); refill_window(); }
    }
  }
#undef LANES
}

}

extern "C" void kernel_launch(void* const* d_in, const int* in_sizes, int n_in, void* d_out, int out_size, void* d_ws, size_t ws_size,
                              hipStream_t stream) {
  Args a;
  a.x0 = (const unsigned char*)d_in[0];
  a.k = (const unsigned char*)d_in[1];
  a.v = (const unsigned char*)d_in[2];
  a.wq = (const unsigned char*)d_in[3];
  a.wo = (const unsigned char*)d_in[4];
  a.wg = (const unsigned char*)d_in[6];
  a.wu = (const unsigned char*)d_in[7];
  a.wd = (const unsigned char*)d_in[8];
  a.out = (unsigned char*)d_out;
  a.ws = (unsigned char*)d_ws;
  (void)hipFuncSetAttribute((const void*)fused_decode_kernel, hipFuncAttributeMaxDynamicSharedMemorySize, LDS_BYTES);
  hipLaunchKernelGGL(fused_decode_kernel, dim3(256), dim3(256), LDS_BYTES, stream, a);
}
```

```cpp
#include <hip/hip_runtime.h>
#include <hip/hip_bf16.h>
#include <stdint.h>
#include <utility>

#pragma clang diagnostic ignored "-Winline-asm"

namespace {

typedef unsigned v4u __attribute__((ext_vector_type(4)));
typedef unsigned v2u __attribute__((ext_vector_type(2)));
typedef float f32x4 __attribute__((ext_vector_type(4)));
typedef float f32x2 __attribute__((ext_vector_type(2)));
typedef __bf16 bf16x2 __attribute__((ext_vector_type(2)));
typedef short v4s __attribute__((ext_vector_type(4)));
#define DI __device__ __forceinline__
#define LAS __attribute__((address_space(3)))
#define ACL "a0", "a1", "a2", "a3", "a4", "a5", "a6", "a7", "a8", "a9", "a10", "a11", "a12", "a13", "a14", "a15", "a16", "a17", "a18", "a19", "a20", "a21", "a22", "a23", "a24", "a25", "a26", "a27", "a28", "a29", "a30", "a31", "a32", "a33", "a34", "a35", "a36", "a37", "a38", "a39", "a40", "a41", "a42", "a43", "a44", "a45", "a46", "a47", "a48", "a49", "a50", "a51", "a52", "a53", "a54", "a55", "a56", "a57", "a58", "a59", "a60", "a61", "a62", "a63"

constexpr int DM = 576, SK = 65536, DF = 4608;
#define XTRA 2
constexpr int NP = 128 - 3 * XTRA;
constexpr int NF = 128 + XTRA;
constexpr int CAP = 64;
constexpr int NVU = (256 - CAP) / 16, NUNITS = NVU + 16;
#define NRV (NUNITS - 4)
#define JQV 12
#define PREPV 3
constexpr int RING = 4, NR = NRV;
constexpr int NL = 4, NW = 5;
constexpr int JQ = JQV;
constexpr int PREP = PREPV;
#define MAPM 1
#define PACE 940
#define FFN_AUX ""
constexpr float C2 = 0.088388347648318440f * 1.4426950408889634f;
constexpr unsigned SPIN_CAP = 1u << 17;

constexpr int LDS_WIN = 0, WIN_DN = 55296, WIN_BYTES = 82944;
constexpr int LDS_RET = WIN_BYTES;
constexpr int LDS_MISC = LDS_RET + 4 * NL * 4096;
constexpr int LDS_XH = LDS_MISC;
constexpr int LDS_ZERO = LDS_MISC + 5120;
constexpr int LDS_XRES = LDS_MISC + 5184;
constexpr int LDS_CMB = LDS_MISC + 5376;
constexpr int LDS_XO = LDS_MISC + 9600;
constexpr int LDS_RED = LDS_MISC + 9856;
constexpr int LDS_ONES = LDS_MISC + 11392;
constexpr int LDS_BYTES = 163840;
static_assert(LDS_ONES + 1152 <= LDS_BYTES && 4 * NW * 4096 <= WIN_BYTES, "LDS map");

constexpr unsigned WS_EPOCH = 0;
constexpr unsigned WS_Q = 4096, Q_BYTES = 1024 * 16;
constexpr unsigned WS_X = WS_Q + 2 * Q_BYTES, X_BYTES = 4096;
constexpr unsigned WS_PART = WS_X + 4 * X_BYTES, PART_H = 80 * 16, PART_BYTES = 256 * 2 * PART_H;
constexpr unsigned WS_YO = WS_PART + 2 * PART_BYTES, YO_BYTES = 36 * 64 * 128;
constexpr unsigned WS_YD = WS_YO + 2 * YO_BYTES, YD_BYTES = 36 * 192 * 128;
constexpr unsigned WS_END = WS_YD + 2 * YD_BYTES;
constexpr unsigned WS_STAMP = (WS_END + 4095u) & ~4095u;

struct Args {
  const unsigned char *x0, *k, *v, *wq, *wo, *wg, *wu, *wd;
  unsigned char *out, *ws;
};

template <int... I, class F> DI void sfor_impl(std::integer_sequence<int, I...>, F&& f) { (f(std::integral_constant<int, I>{}), ...); }
template <int N, class F> DI void sfor(F&& f) { sfor_impl(std::make_integer_sequence<int, N>{}, f); }

template <int CTRL> DI float dppf(float x) { return __builtin_bit_cast(float, __builtin_amdgcn_mov_dpp(__builtin_bit_cast(int, x), CTRL, 0xf, 0xf, true)); }
template <int CTRL> DI unsigned dppu(unsigned x) { return (unsigned)__builtin_amdgcn_mov_dpp((int)x, CTRL, 0xf, 0xf, true); }
constexpr int QP0 = 0x00, QP1 = 0x55, QP2 = 0xAA, QP3 = 0xFF, XOR1 = 0xB1, XOR2 = 0x4E, HMIR = 0x141, ROR4 = 0x124, ROR8 = 0x128, SHR4 = 0x114, SHL4 = 0x104;
DI unsigned cvtpk(float lo, float hi) { f32x2 v = {lo, hi}; return __builtin_bit_cast(unsigned, __builtin_convertvector(v, bf16x2)); }
DI float bflo(unsigned u) { return __builtin_bit_cast(float, u << 16); }
DI float bfhi(unsigned u) { return __builtin_bit_cast(float, u & 0xffff0000u); }
DI float dot2(unsigned a, unsigned b, float c) { return __builtin_amdgcn_fdot2_f32_bf16(__builtin_bit_cast(bf16x2, a), __builtin_bit_cast(bf16x2, b), c, false); }
DI float rbf(float x) { return bflo(cvtpk(x, x)); }
DI float xrow_max(float x) {
  auto s = __builtin_amdgcn_permlane16_swap(__float_as_uint(x), __float_as_uint(x), false, false);
  x = fmaxf(__uint_as_float(s[0]), __uint_as_float(s[1]));
  auto t = __builtin_amdgcn_permlane32_swap(__float_as_uint(x), __float_as_uint(x), false, false);
  return fmaxf(__uint_as_float(t[0]), __uint_as_float(t[1]));
}
DI float xrow_sum(float x) {
  auto s = __builtin_amdgcn_permlane16_swap(__float_as_uint(x), __float_as_uint(x), false, false);
  x = __uint_as_float(s[0]) + __uint_as_float(s[1]);
  auto t = __builtin_amdgcn_permlane32_swap(__float_as_uint(x), __float_as_uint(x), false, false);
  return __uint_as_float(t[0]) + __uint_as_float(t[1]);
}
DI float wave_sum(float x) {
  x += dppf<XOR1>(x); x += dppf<XOR2>(x); x += dppf<ROR4>(x); x += dppf<ROR8>(x);
  return xrow_sum(x);
}
DI void wg_barrier() { asm volatile("s_waitcnt lgkmcnt(0)\n\ts_barrier" ::: "memory", ACL); }
template <int N> DI void vmwait() { asm volatile("s_waitcnt vmcnt(%c0)" ::"i"(N) : "memory", ACL); }
DI void lgkwait() { asm volatile("s_waitcnt lgkmcnt(0)" ::: "memory", ACL); }

#define LDSP(T, off) ((LAS T*)(uintptr_t)(off))
DI v4u lds_r128(unsigned a) { return *LDSP(v4u, a); }
DI v2u lds_tr(unsigned a) { return __builtin_bit_cast(v2u, __builtin_amdgcn_ds_read_tr16_b64_v4i16(LDSP(v4s, a))); }
DI v4u lds_frag(unsigned a, unsigned second) { const v2u lo = lds_tr(a), hi = lds_tr(a + second); return v4u{lo.x, lo.y, hi.x, hi.y}; }

typedef __amdgpu_buffer_rsrc_t rsrc_t;
DI rsrc_t mk_rsrc(const unsigned char* p, unsigned bytes) { return __builtin_amdgcn_make_buffer_rsrc((void*)p, 0, bytes, 0x00020000); }
DI int lane_id() { int l; asm volatile("v_mbcnt_lo_u32_b32 %0, -1, 0\n\tv_mbcnt_hi_u32_b32 %0, -1, %0" : "=v"(l)::ACL); return l; }
DI unsigned opqs(unsigned x) { asm volatile("" : "+s"(x)); return x; }

DI void glds_b(unsigned voff, rsrc_t r, unsigned soff, unsigned lds_dst) {
  unsigned keep;
  asm volatile("s_mov_b32 %0, m0\n\ts_mov_b32 m0, %4\n\ts_nop 0\n\tbuffer_load_dwordx4 %1, %2, %3 offen nt lds\n\ts_mov_b32 m0, %0"
               : "=&s"(keep) : "v"(voff), "s"(r), "s"(soff), "s"(lds_dst) : "memory");
}
DI void glds_v48(const unsigned char* vaddr, unsigned lds_dst) {
  unsigned keep;
  asm volatile("s_mov_b32 %0, m0\n\ts_mov_b32 m0, %2\n\ts_mov_b32 exec_hi, 0xffff\n\tglobal_load_lds_dwordx4 %1, off " FFN_AUX "\n\ts_mov_b32 exec_hi, -1\n\ts_mov_b32 m0, %0"
               : "=&s"(keep) : "v"(vaddr), "s"(lds_dst) : "memory");
}
struct KV { rsrc_t k, v; unsigned koff, voff, so; };
DI void glds_unit(KV& s, unsigned lds_dst) {
  unsigned keep;
  asm volatile("s_mov_b32 %0, m0\n\ts_mov_b32 m0, %6\n\ts_nop 0\n\t"
               "buffer_load_dwordx4 %2, %3, %1 offen nt lds\n\tbuffer_load_dwordx4 %2, %3, %1 offen offset:1024 nt lds\n\t"
               "s_add_u32 m0, m0, 2048\n\ts_nop 0\n\t"
               "buffer_load_dwordx4 %4, %5, %1 offen nt lds\n\tbuffer_load_dwordx4 %4, %5, %1 offen offset:1024 nt lds\n\ts_mov_b32 m0, %0\n\ts_add_u32 %1, %1, 8192"
               : "=&s"(keep), "+s"(s.so) : "v"(s.koff), "s"(s.k), "v"(s.voff), "s"(s.v), "s"(lds_dst) : "memory", "scc");
}

template <int U> DI void unit_load(KV& s) {
  constexpr int B = U < NVU ? CAP + 16 * U : 16 * (U - NVU);
#define UL(R) asm volatile("buffer_load_dwordx4 " R "[%c5:%c6], %1, %2, %0 offen nt\n\tbuffer_load_dwordx4 " R "[%c7:%c8], %1, %2, %0 offen offset:1024 nt\n\t" \
                           "buffer_load_dwordx4 " R "[%c9:%c10], %3, %4, %0 offen nt\n\tbuffer_load_dwordx4 " R "[%c11:%c12], %3, %4, %0 offen offset:1024 nt\n\t" \
                           "s_add_u32 %0, %0, 8192" \
                           : "+s"(s.so) : "v"(s.koff), "s"(s.k), "v"(s.voff), "s"(s.v), "i"(B), "i"(B + 3), "i"(B + 4), "i"(B + 7), "i"(B + 8), "i"(B + 11), "i"(B + 12), "i"(B + 15) : "memory", "scc")
  if constexpr (U < NVU) UL("v"); else UL("a");
#undef UL
}
template <int U> DI void unit_to_lds(unsigned a) {
  constexpr int B = U < NVU ? CAP + 16 * U : 16 * (U - NVU);
#define UW(R) asm volatile("ds_write_b128 %0, " R "[%c1:%c2]\n\tds_write_b128 %0, " R "[%c3:%c4] offset:1024\n\t" \
                           "ds_write_b128 %0, " R "[%c5:%c6] offset:2048\n\tds_write_b128 %0, " R "[%c7:%c8] offset:3072\n\ts_waitcnt lgkmcnt(0)" \
                           ::"v"(a), "i"(B), "i"(B + 3), "i"(B + 4), "i"(B + 7), "i"(B + 8), "i"(B + 11), "i"(B + 12), "i"(B + 15) : "memory")
  if constexpr (U < NVU) UW("v"); else UW("a");
#undef UW
}
template <int U> DI void unit_from_lds(unsigned a) {
  constexpr int B = U < NVU ? CAP + 16 * U : 16 * (U - NVU);
#define UR(R) asm volatile("ds_read_b128 " R "[%c1:%c2], %0\n\tds_read_b128 " R "[%c3:%c4], %0 offset:1024\n\t" \
                           "ds_read_b128 " R "[%c5:%c6], %0 offset:2048\n\tds_read_b128 " R "[%c7:%c8], %0 offset:3072" \
                           ::"v"(a), "i"(B), "i"(B + 3), "i"(B + 4), "i"(B + 7), "i"(B + 8), "i"(B + 11), "i"(B + 12), "i"(B + 15) : "memory")
  if constexpr (U < NVU) UR("v"); else UR("a");
#undef UR
}

struct Att { v4u q0, q1; unsigned msk[4]; f32x4 acc; float m, l; };

template <int U> DI void unit_proc(Att& s) {
  constexpr int B = U < NVU ? CAP + 16 * U : 16 * (U - NVU);
  f32x4 d;
#define QK(R) asm volatile("s_nop 1\n\tv_mfma_f32_16x16x32_bf16 %0, " R "[%c3:%c4], %1, 0\n\tv_mfma_f32_16x16x32_bf16 %0, " R "[%c5:%c6], %2, %0\n\ts_nop 7" \
                           : "=&v"(d) : "v"(s.q0), "v"(s.q1), "i"(B), "i"(B + 3), "i"(B + 4), "i"(B + 7) : "memory", ACL)
  if constexpr (U < NVU) QK("v"); else QK("a");
#undef QK
  const float sc = ((dppf<QP0>(d[0]) + dppf<QP1>(d[1])) + (dppf<QP2>(d[2]) + dppf<QP3>(d[3]))) * C2;
  const float mx = xrow_max(fmaxf(sc, dppf<HMIR>(sc)));
  const float mn = fmaxf(s.m, mx);
  const float al = __builtin_amdgcn_exp2f(s.m - mn);
  const float p = __builtin_amdgcn_exp2f(sc - mn);
  s.m = mn; s.l = s.l * al + p;
  s.acc *= al;
  const unsigned pp = cvtpk(p, p), pl = dppu<SHR4>(pp), pr = dppu<SHL4>(pp);
  const v4u p0 = {pp & s.msk[0], pp & s.msk[1], pl & s.msk[2], pl & s.msk[3]};
  const v4u p1 = {pr & s.msk[0], pr & s.msk[1], pp & s.msk[2], pp & s.msk[3]};
#define PV(R) asm volatile("s_nop 1\n\tv_mfma_f32_16x16x32_bf16 %0, " R "[%c4:%c5], %2, %0\n\tv_mfma_f32_16x16x32_bf16 %0, " R "[%c6:%c7], %3, %0\n\ts_nop 7" \
                           : "+v"(s.acc), "+v"(s.l) : "v"(p0), "v"(p1), "i"(B + 8), "i"(B + 11), "i"(B + 12), "i"(B + 15) : "memory", ACL)
  if constexpr (U < NVU) PV("v"); else PV("a");
#undef PV
}

DI void mfma1(f32x4& d, v4u a, v4u b) { asm volatile("s_nop 1\n\tv_mfma_f32_16x16x32_bf16 %0, %1, %2, %0" : "+v"(d) : "v"(a), "v"(b)); }
DI void mfma2(f32x4& d, v4u a0, v4u b0, v4u a1, v4u b1) {
  asm volatile("s_nop 1\n\tv_mfma_f32_16x16x32_bf16 %0, %1, %2, %0\n\tv_mfma_f32_16x16x32_bf16 %0, %3, %4, %0" : "+v"(d) : "v"(a0), "v"(b0), "v"(a1), "v"(b1));
}
DI void mfma3(f32x4& d0, f32x4& d1, f32x4& d2, v4u a, v4u b0, v4u b1, v4u b2) {
  asm volatile("s_nop 1\n\tv_mfma_f32_16x16x32_bf16 %0, %3, %4, %0\n\tv_mfma_f32_16x16x32_bf16 %1, %3, %5, %1\n\tv_mfma_f32_16x16x32_bf16 %2, %3, %6, %2"
               : "+v"(d0), "+v"(d1), "+v"(d2) : "v"(a), "v"(b0), "v"(b1), "v"(b2));
}
DI void mfma_fence(f32x4& d) { asm volatile("s_nop 7" : "+v"(d)::ACL); }

#define STAMP(i) do {} while (0)
#define STAMP_INIT() do {} while (0)
#define STAMP_DUMP() do {} while (0)

#define SB() __builtin_amdgcn_sched_barrier(0)

__global__ void __launch_bounds__(256, 1) __attribute__((amdgpu_num_vgpr(CAP)))
fused_decode_kernel(Args a) {
  extern __shared__ __attribute__((aligned(16))) unsigned char lds_image[];
  asm volatile("" ::"v"((unsigned)(uintptr_t)(LAS unsigned char*)lds_image) : "v255", "a255");
  const unsigned w0 = __builtin_amdgcn_readfirstlane(threadIdx.x >> 6);
  const bool isP = ((blockIdx.x >> 3) & 3) == 0;
  const bool isR = isP && (blockIdx.x >> 5) * 8 + (blockIdx.x & 7) < 36;
  const int N = isP ? NP : NF;
#define ATT_IDS kvh = 2 * (bi >> 2) + (xcd >> 2), sp = 4 * (xcd & 3) + (bi & 3)
#define LANES const int lane = lane_id(), g4 = lane >> 4, c16 = lane & 15; const int w = opqs(w0), bx = opqs(blockIdx.x), tid = w * 64 + lane; \
  const int xcd = bx & 7, bi = bx >> 3, ATT_IDS, pidx = 8 * xcd + (bi >> 2), fidx = 24 * xcd + (bi - (bi >> 2) - 1), ridx = (bi >> 2) * 8 + xcd; \
  (void)g4; (void)c16; (void)w; (void)tid; (void)pidx; (void)fidx; (void)ridx; (void)kvh; (void)sp

  const rsrc_t rs = mk_rsrc(a.ws, WS_END);
  KV kv;
  {
    LANES;
    const int nP_before = (sp + 3) >> 2;
    const int row0 = nP_before * NP + (sp - nP_before) * NF;
    kv.k = mk_rsrc(a.k + ((size_t)kvh * SK + (size_t)row0 * 32) * 256, N * 8192);
    kv.v = mk_rsrc(a.v + ((size_t)kvh * SK + (size_t)row0 * 32) * 256, N * 8192);
    kv.so = 0;
  }
  auto kv_lanes = [&]() {
    LANES;
    kv.koff = w * 2048 + (c16 >> 2) * 256 + g4 * 64 + (c16 & 3) * 16;
    kv.voff = w * 2048 + lane * 16;
  };
  kv_lanes();
  STAMP_INIT();
  STAMP(0);

  auto load_wq = [&]() {
    LANES;
    const rsrc_t r = mk_rsrc(a.wq + pidx * 128, DM * 8192 - pidx * 128);
    const unsigned o = (lane >> 3) * 8192 + (((lane & 7) ^ (((lane >> 4) & 3) << 1)) << 4);
#pragma unroll 1
    for (int pc = w; pc < 72; pc += 4) glds_b(o, r, pc * 65536, LDS_WIN + pc * 1024);
  };
  auto load_rows = [&](bool down) {
    LANES;
    const int npieces = down ? 27 : 72;
    const unsigned dst = down ? LDS_WIN + WIN_DN : LDS_WIN;
    const rsrc_t r = mk_rsrc(down ? a.wd + (size_t)fidx * 24 * 1152 : a.wo + (size_t)pidx * 64 * 1152, npieces * 1024);
#pragma unroll 1
    for (int pc = w; pc < npieces; pc += 4) {
      const unsigned idx = pc * 64 + lane, row = idx / 72u, cc = idx - row * 72u;
      glds_b(row * 1152 + ((cc ^ (((row >> 1) & 3) << 1)) << 4), r, 0, dst + pc * 1024);
    }
  };
  auto load_ffn = [&]() {
    LANES;
    const int l48 = lane < 48 ? lane : 0, r = l48 / 6, cc = l48 - r * 6;
    const uint64_t gb = (uint64_t)a.wg, ub = (uint64_t)(a.wu - 48);
    const unsigned off = cc * 16 + fidx * 48 + (8 * w + r) * (DF * 2);
    unsigned lo = (cc < 3 ? (unsigned)gb : (unsigned)ub) + off, hi = (cc < 3 ? (unsigned)(gb >> 32) : (unsigned)(ub >> 32)) + (lo < off ? 1u : 0u);
#pragma unroll 1
    for (int pc = w; pc < 72; pc += 4) {
      glds_v48(__builtin_bit_cast(const unsigned char*, v2u{lo, hi}), LDS_WIN + pc * 768);
      lo += 32 * DF * 2; hi += lo < 32 * DF * 2 ? 1u : 0u;
    }
    load_rows(true);
  };
  auto refill_window = [&]() {
    const int w = opqs(w0);
    kv_lanes();
    kv.so = opqs((NR + NL) * 8192);
#pragma unroll 1
    for (int i = 0; i < NW; ++i) glds_unit(kv, LDS_WIN + (w * NW + i) * 4096);
  };

  const unsigned ep = *(const __attribute__((address_space(4))) unsigned*)(a.ws + WS_EPOCH);
  unsigned xres0;
  auto xh_addr = [&](int w, int slot) { return LDS_XH + w * 1280 + ((slot & ~7) + (slot & 3) * 2 + ((slot >> 2) & 1)) * 8; };
  v2u x0r[3];
  {
    LANES;
    if (isP) {
      const rsrc_t rx = mk_rsrc(a.x0, DM * 2);
      asm volatile("buffer_load_dwordx2 %0, %4, %6, 0 offen\n\tbuffer_load_dwordx2 %1, %4, %6, 0 offen offset:512\n\t"
                   "buffer_load_dwordx2 %2, %4, %6, 0 offen offset:1024\n\tbuffer_load_ushort %3, %5, %6, 0 offen"
                   : "=&v"(x0r[0]), "=&v"(x0r[1]), "=&v"(x0r[2]), "=&v"(xres0) : "v"(lane * 8), "v"((16 * ridx + c16) * 2), "s"(rx) : "memory");
      load_wq();
    }
    if (tid < 16) *LDSP(unsigned, LDS_ZERO + tid * 4) = 0u;
    if (tid < 144) *LDSP(v2u, LDS_ONES + tid * 8) = v2u{0x3F803F80u, 0x3F803F80u};
  }
  sfor<3>([&](auto I) { unit_load<RING + I.value>(kv); });
  if (isP) {
    sfor<PREP - 3>([&](auto I) { unit_load<RING + 3 + I.value>(kv); });
    asm volatile("s_waitcnt vmcnt(%c4)" : "+v"(x0r[0]), "+v"(x0r[1]), "+v"(x0r[2]), "+v"(xres0) : "i"(18 + 4 * PREP) : "memory");
    LANES;
#pragma unroll
    for (int i = 0; i < 3; ++i) if (i < 2 || lane < 16) *LDSP(v2u, xh_addr(w, lane + 64 * i)) = x0r[i];
    if (tid < 16) *LDSP(unsigned, LDS_XRES + tid * 4) = xres0;
  }

  const unsigned tagA = ep + 1u, tagB = (ep + 1u) * 0x9E3779B1u + 0x7F4A7C15u;
  bool dead = false;
  auto ok4 = [&](const v4u& v) { return v.y == tagA && v.w == tagB; };
  auto put = [&](unsigned off, unsigned v0, unsigned v1) { __builtin_amdgcn_raw_buffer_store_b128(v4u{v0, tagA, v1, tagB}, rs, off, 0, 16); };
  auto get = [&](unsigned off) { return __builtin_amdgcn_raw_buffer_load_b128(rs, off, 0, 16); };
  unsigned pace_t = 0;
  auto pace_start = [&]() { if (PACE) pace_t = (unsigned)__builtin_amdgcn_s_memrealtime() * 16u; };
  auto pace = [&]() {
    if (PACE) {
      pace_t += PACE;
      while ((int)((unsigned)__builtin_amdgcn_s_memrealtime() * 16u - pace_t) < 0) __builtin_amdgcn_s_sleep(1);
    }
  };
  pace_start();

  auto stage_x = [&](v2u (&xr)[3], int w, int lane, float& s1, float& s2) {
#pragma unroll
    for (int i = 0; i < 3; ++i) if (i < 2 || lane < 16) *LDSP(v2u, xh_addr(w, lane + 64 * i)) = xr[i];
    if (lane >= 16) xr[2] = v2u{0u, 0u};
    s1 = 0.f; s2 = 0.f;
#pragma unroll
    for (int i = 0; i < 3; ++i) { s1 = dot2(xr[i].x, 0x3F803F80u, s1); s2 = dot2(xr[i].x, xr[i].x, s2); s1 = dot2(xr[i].y, 0x3F803F80u, s1); s2 = dot2(xr[i].y, xr[i].y, s2); }
    asm volatile("s_waitcnt lgkmcnt(0)" : "+v"(s1), "+v"(s2)::"memory", ACL);
  };
  auto ln_stats = [&](float s1, float s2, float& mean, float& inv) {
    s1 = wave_sum(s1); s2 = wave_sum(s2);
    const float md = s1 * (1.f / DM);
    mean = rbf(md);
    const float var = rbf(fmaxf(s2 * (1.f / DM) - md * md, 0.f));
    inv = __builtin_amdgcn_rcpf(rbf(__builtin_amdgcn_sqrtf(rbf(var + 1e-5f))));
    asm volatile("" : "+v"(mean), "+v"(inv));
  };
  auto gather_x = [&](v2u (&xr)[3], unsigned base, int lane) {
    for (unsigned spin = 0;; ++spin) {
      asm volatile("" ::: "memory");
      v4u t[3];
#pragma unroll
      for (int i = 0; i < 3; ++i) t[i] = get(base + ((i < 2 || lane < 16) ? (lane + 64 * i) : lane) * 16);
#pragma unroll
      for (int i = 0; i < 3; ++i) xr[i] = v2u{t[i].x, t[i].z};
      if (__all(ok4(t[0]) && ok4(t[1]) && ok4(t[2])) || dead) break;
      if (spin > SPIN_CAP) { dead = true; break; }
    }
  };
  struct Proj { unsigned wa, second; };
  auto proj_setup = [&](auto KSc, int w, int lane) {
    constexpr int KS = KSc.value;
    const int g4 = lane >> 4, c16 = lane & 15;
    const unsigned hl = (2 * g4 + (c16 >> 3)) & 3, trow = 4 * g4 + (c16 >> 2);
    return Proj{(KS == 2 ? LDS_WIN : LDS_WIN + WIN_DN) + trow * 1152 + (((unsigned)w ^ hl) << 5) + (lane & 3) * 8,
                (KS == 1 && g4 >= 2) ? 8u * 1152 : 16u * 1152};
  };
  auto proj_frag = [&](auto KSc, const Proj& p, auto Nc) {
    constexpr int KS = KSc.value, n = Nc.value, tl = n / KS, ks = n % KS;
    constexpr int i = tl < 3 ? 4 * tl : (tl < 5 ? 1 + 4 * (tl - 3) : (tl < 7 ? 2 + 4 * (tl - 5) : 3 + 4 * (tl - 7)));
    return lds_frag(p.wa + i * 128 + ks * (32 * 1152), p.second);
  };
  auto proj_run = [&](auto KSc, auto PREc, const Proj& p, v4u* wf, unsigned ybase) {
    constexpr int KS = KSc.value, PRE = PREc.value, NFR = 9 * KS;
    LANES;
    const unsigned xsrc = KS == 2 ? LDS_XO : LDS_XO + w * 64, nprod = KS == 2 ? 64 : 192, prod = KS == 2 ? pidx : fidx;
    f32x4 d[3] = {{0, 0, 0, 0}, {0, 0, 0, 0}, {0, 0, 0, 0}};
    v4u av[2][KS];
    auto rdA = [&](int G) {
#pragma unroll
      for (int k = 0; k < KS; ++k) av[G & 1][k] = lds_r128((c16 >> 2) == G ? xsrc + 64 * k + g4 * 16 : LDS_ZERO);
    };
    rdA(0); rdA(1);
    SB();
    sfor<9>([&](auto Tc) {
      constexpr int tl = Tc.value, G = tl < 3 ? 0 : (tl < 5 ? 1 : (tl < 7 ? 2 : 3)), u = tl < 3 ? tl : ((tl - 3) & 1);
      if constexpr (KS == 2) mfma2(d[u], av[G & 1][0], wf[(2 * tl) % PRE], av[G & 1][1], wf[(2 * tl + 1) % PRE]);
      else mfma1(d[u], av[G & 1][0], wf[tl % PRE]);
      sfor<KS>([&](auto Kc) {
        constexpr int n = KS * tl + Kc.value + PRE;
        if constexpr (n < NFR) wf[n % PRE] = proj_frag(KSc, p, std::integral_constant<int, n>{});
      });
      if constexpr (tl == 2) rdA(2);
      if constexpr (tl == 4) rdA(3);
      SB();
    });
#pragma unroll
    for (int u = 0; u < 3; ++u) {
      mfma_fence(d[u]);
      const float nb = dppf<XOR1>(d[u][0]);
      const unsigned T = w + 4 * (4 * u + g4);
      if (!(lane & 1) && (u < 2 || g4 == 0)) put(ybase + (T * nprod + prod) * 128 + (c16 >> 1) * 16, __float_as_uint(d[u][0]), __float_as_uint(nb));
    }
  };
  auto reduce_cols = [&](auto NIc, unsigned ybase) {
    constexpr int NI = NIc.value;
    LANES;
    float s0 = 0.f, s1 = 0.f;
    for (unsigned spin = 0;; ++spin) {
      asm volatile("" ::: "memory");
      v4u t[NI];
#pragma unroll
      for (int i = 0; i < NI; ++i) t[i] = get(ybase + ridx * (NI * 4096) + i * 4096 + tid * 16);
      bool ok = true; s0 = 0.f; s1 = 0.f;
#pragma unroll
      for (int i = 0; i < NI; ++i) { ok = ok && ok4(t[i]); s0 += __uint_as_float(t[i].x); s1 += __uint_as_float(t[i].z); }
      if (__all(ok) || dead) break;
      if (spin > SPIN_CAP) { dead = true; break; }
    }
    s0 += dppf<ROR8>(s0); s1 += dppf<ROR8>(s1);
    s0 = xrow_sum(s0); s1 = xrow_sum(s1);
    if (lane < 16) *LDSP(float, LDS_RED + ((lane & 7) * 2 + (lane >> 3)) * 16 + w * 4) = lane < 8 ? s0 : s1;
    const unsigned xold = *LDSP(unsigned, LDS_XRES + c16 * 4);
    wg_barrier();
    const f32x4 r = *LDSP(f32x4, LDS_RED + c16 * 16);
    const float y = (r[0] + r[1]) + (r[2] + r[3]);
    const unsigned xres = cvtpk(bflo(xold) + rbf(y), 0.f) & 0xffffu;
    if (tid < 16) *LDSP(unsigned, LDS_XRES + tid * 4) = xres;
    return v2u{xres | (dppu<QP1>(xres) << 16), dppu<QP2>(xres) | (dppu<QP3>(xres) << 16)};
  };
  constexpr std::integral_constant<int, 1> I1{};
  constexpr std::integral_constant<int, 2> I2{};

#pragma unroll 1
  for (int it = 0; it < 2; ++it) {
    if (isP) {
      constexpr int PRE = 7;
      LANES;
      if (it == 0) vmwait<4 * PREP>(); else vmwait<0>();
      wg_barrier();
      const unsigned hl = (2 * g4 + (c16 >> 3)) & 3, trow = 4 * g4 + (c16 >> 2);
      const unsigned wa = LDS_WIN + trow * 128 + (((unsigned)w ^ hl) << 5) + (lane & 3) * 8;
      v4u wf[PRE];
#pragma unroll
      for (int i = 0; i < PRE; ++i) wf[i] = lds_frag(wa + i * 4096, 2048);
      v2u xr[3];
      if (it == 1) gather_x(xr, WS_X + 1 * X_BYTES, lane);
      else {
#pragma unroll
        for (int i = 0; i < 3; ++i) xr[i] = *LDSP(v2u, xh_addr(w, (i < 2 || lane < 16) ? lane + 64 * i : lane));
      }
      STAMP(1 + 16 * it);
      float s1, s2;
      stage_x(xr, w, lane, s1, s2);
      const unsigned xa = (c16 < 8 ? LDS_XH + w * 1280 : LDS_ONES) + g4 * 16;
      f32x4 d0 = {0, 0, 0, 0}, d1 = {0, 0, 0, 0};
      v4u av[3];
#pragma unroll
      for (int i = 0; i < 3; ++i) av[i] = lds_r128(xa + i * 64);
      float mean, inv;
      SB();
      ln_stats(s1, s2, mean, inv);
      SB();
      sfor<18>([&](auto Kc) {
        constexpr int ks = Kc.value;
        mfma1((ks & 1) ? d1 : d0, av[ks % 3], wf[ks % PRE]);
        if constexpr (ks + 3 < 18) av[ks % 3] = lds_r128(xa + (ks + 3) * 64);
        if constexpr (ks + PRE < 18) wf[ks % PRE] = lds_frag(wa + (ks + PRE) * 4096, 2048);
        SB();
      });
      mfma_fence(d0); mfma_fence(d1);
      const unsigned sv = __float_as_uint(d0[0] + d1[0]);
      auto sw = __builtin_amdgcn_permlane32_swap(sv, sv, false, false);
      const unsigned qb = cvtpk((__uint_as_float(sw[0]) - mean * __uint_as_float(sw[1])) * inv, 0.f) & 0xffffu;
      const unsigned v0 = qb | (dppu<QP1>(qb) << 16), v1 = dppu<QP2>(qb) | (dppu<QP3>(qb) << 16);
      if (lane < 16 && !(lane & 3)) put(WS_Q + it * Q_BYTES + (pidx * 16 + w * 4 + (lane >> 2)) * 16, v0, v1);
      STAMP(2 + 16 * it);
      if (it == 1) { wg_barrier(); refill_window(); }
    }

    Att s;
    unsigned qoff, lret, lwin;
    kv_lanes();
    {
      LANES;
#pragma unroll
      for (int i = 0; i < 4; ++i) s.msk[i] = ((c16 & 7) >> 1) == i ? ((c16 & 1) ? 0xffff0000u : 0xffffu) : 0u;
      qoff = WS_Q + it * Q_BYTES + ((2 * kvh + (c16 >> 3)) * 32 + 2 * (4 * g4 + (c16 & 3))) * 16;
      lret = LDS_RET + w * (NL * 4096) + lane * 16; lwin = LDS_WIN + w * (NW * 4096) + lane * 16;
    }
    auto q_issue = [&](v4u& t0, v4u& t1) {
      asm volatile("buffer_load_dwordx4 %0, %2, %3, 0 offen sc1\n\tbuffer_load_dwordx4 %1, %2, %3, 0 offen offset:16 sc1" : "=&v"(t0), "=&v"(t1) : "v"(qoff), "s"(rs) : "memory");
    };
    auto q_finish = [&](v4u& t0, v4u& t1) {
      for (unsigned spin = 0; !(__all(ok4(t0) && ok4(t1)) || dead); ++spin) {
        if (spin > SPIN_CAP) { dead = true; break; }
        q_issue(t0, t1);
        asm volatile("s_waitcnt vmcnt(0)" : "+v"(t0), "+v"(t1)::"memory");
      }
      const v4u q = {t0.x, t0.z, t1.x, t1.z}, z = {0, 0, 0, 0};
      const bool hi = (lane_id() & 4) != 0;
      s.q0 = hi ? z : q; s.q1 = hi ? q : z;
      s.acc = f32x4{0, 0, 0, 0}; s.m = -3.0e38f; s.l = 0.f;
    };
    if (it == 0) {
      if (!isP) sfor<PREP - 3>([&](auto I) { vmwait<8>(); unit_load<RING + 3 + I.value>(kv); });
      sfor<JQ - PREP>([&](auto I) { vmwait<8>(); unit_load<RING + PREP + I.value>(kv); });
      v4u t0, t1;
      vmwait<8>();
      q_issue(t0, t1);
      unit_load<RING + JQ>(kv);
      asm volatile("s_waitcnt vmcnt(4)" : "+v"(t0), "+v"(t1)::"memory");
      q_finish(t0, t1);
      pace_t += (JQ - 2) * PACE;
      STAMP(3);
      constexpr int REM = (NP - NR - NL) & 3, JS = NR + NL + REM + 4;
      static_assert(2 * (NR + 3 - JQ - 1) >= NR, "backlog must be gone before the ring wraps");
      sfor<JS - JQ - 1>([&](auto I) {
        constexpr int j = JQ + 1 + I.value;
        vmwait<8>();
        pace();
        if constexpr (j < NR) unit_load<RING + j>(kv); else unit_load<(j - NR) & 3>(kv);
        constexpr int done = (2 * I.value < j - 3) ? 2 * I.value : j - 3;
        constexpr int upto = (2 * I.value + 2 < j - 2) ? 2 * I.value + 2 : j - 2;
        sfor<upto - done>([&](auto K) {
          constexpr int u = done + K.value;
          if constexpr (u < NR) unit_proc<RING + u>(s);
          else { unit_proc<(u - NR) & 3>(s); if constexpr (u < NR + NL) unit_to_lds<(u - NR) & 3>(lret + (u - NR) * 4096); }
        });
      });
#pragma unroll 1
      for (int j = JS; j < N; j += 4) {
        sfor<4>([&](auto I) {
          vmwait<8>();
          pace();
          unit_load<(JS + I.value - NR) & 3>(kv);
          unit_proc<(JS + I.value - 3 - NR) & 3>(s);
        });
      }
      if (isP) {
        load_rows(false);
        vmwait<26>(); unit_proc<(JS - 3 - NR) & 3>(s);
        vmwait<22>(); unit_proc<(JS - 2 - NR) & 3>(s);
        vmwait<18>(); unit_proc<(JS - 1 - NR) & 3>(s);
      } else {
        vmwait<8>(); unit_proc<(JS - 3 - NR) & 3>(s);
        vmwait<4>(); unit_proc<(JS - 2 - NR) & 3>(s);
        vmwait<0>(); unit_proc<(JS - 1 - NR) & 3>(s);
      }
    } else {
      v4u t0, t1;
      q_issue(t0, t1);
      asm volatile("s_waitcnt vmcnt(0)" : "+v"(t0), "+v"(t1)::"memory");
      q_finish(t0, t1);
      pace_start();
      STAMP(19);
      constexpr int NON = NR + NL + NW;
      constexpr int T1 = 3 + NON + ((NP - RING - NON - 3 - NON) & 3);
      sfor<4>([&](auto I) {
        unit_proc<I.value>(s);
        if constexpr (I.value < 3) unit_load<I.value>(kv);
      });
      sfor<T1 - 3>([&](auto I) {
        constexpr int t = 3 + I.value, o = I.value;
        if constexpr (o >= NR && o < NR + NL) unit_from_lds<RING + (o & 1)>(lret + (o - NR) * 4096);
        if constexpr (o < NR) unit_proc<RING + o>(s);
        vmwait<8>();
        pace();
        unit_load<t & 3>(kv);
        if constexpr (o >= NR + NL && o < NON) unit_from_lds<RING + (o & 1)>(lwin + (o - NR - NL) * 4096);
        unit_proc<(t - 3) & 3>(s);
        if constexpr (o >= NR && o < NON) { lgkwait(); unit_proc<RING + (o & 1)>(s); }
      });
#pragma unroll 1
      for (int t = T1; t < N - RING - NON; t += 4) {
        sfor<4>([&](auto I) {
          vmwait<8>();
          pace();
          unit_load<(T1 + I.value) & 3>(kv);
          unit_proc<(T1 + I.value - 3) & 3>(s);
        });
      }
      if (isP) {
        wg_barrier();
        load_rows(false);
        vmwait<26>(); unit_proc<(T1 - 3) & 3>(s);
        vmwait<22>(); unit_proc<(T1 - 2) & 3>(s);
        vmwait<18>(); unit_proc<(T1 - 1) & 3>(s);
      } else {
        vmwait<8>(); unit_proc<(T1 - 3) & 3>(s);
        vmwait<4>(); unit_proc<(T1 - 2) & 3>(s);
        vmwait<0>(); unit_proc<(T1 - 1) & 3>(s);
      }
    }
    STAMP((isP ? 14 : 4) + 16 * it);

    {
      LANES;
      float l = s.l + dppf<HMIR>(s.l);
      l = xrow_sum(l);
      *LDSP(f32x4, LDS_CMB + w * 1024 + lane * 16) = s.acc;
      if (lane == 0 || lane == 8) *LDSP(f32x2, LDS_CMB + 4096 + lane * 4 + w * 8) = f32x2{s.m, l};
      wg_barrier();
      if (tid < 128) {
        const int hh = tid >> 6, e = tid & 63;
        const f32x4 ml0 = *LDSP(f32x4, LDS_CMB + 4096 + hh * 32), ml1 = *LDSP(f32x4, LDS_CMB + 4096 + hh * 32 + 16);
        f32x2 o[4];
#pragma unroll
        for (int i = 0; i < 4; ++i) o[i] = *LDSP(f32x2, LDS_CMB + i * 1024 + (16 * (e >> 4) + 8 * hh + (e & 7)) * 16 + ((e >> 3) & 1) * 8);
        const float M = fmaxf(fmaxf(ml0[0], ml0[2]), fmaxf(ml1[0], ml1[2]));
        const float f0 = __builtin_amdgcn_exp2f(ml0[0] - M), f1 = __builtin_amdgcn_exp2f(ml0[2] - M), f2 = __builtin_amdgcn_exp2f(ml1[0] - M), f3 = __builtin_amdgcn_exp2f(ml1[2] - M);
        const float o0 = (f0 * o[0].x + f1 * o[1].x) + (f2 * o[2].x + f3 * o[3].x), o1 = (f0 * o[0].y + f1 * o[1].y) + (f2 * o[2].y + f3 * o[3].y);
        const float L = (f0 * ml0[1] + f1 * ml0[3]) + (f2 * ml1[1] + f3 * ml1[3]);
        const unsigned pb = WS_PART + it * PART_BYTES + ((kvh * 16 + sp) * 2 + hh) * PART_H;
        put(pb + e * 16, __float_as_uint(o0), __float_as_uint(o1));
        if (e == 0) put(pb + 64 * 16, __float_as_uint(M), __float_as_uint(L));
      }
    }
    STAMP(5 + 16 * it);

    if (isP) {
      constexpr int PRE = 6;
      v4u wf[PRE];
      Proj pj;
      {
        LANES;
        vmwait<0>();
        wg_barrier();
        pj = proj_setup(I2, w, lane);
        sfor<PRE>([&](auto Nc) { wf[Nc.value] = proj_frag(I2, pj, Nc); });
        const int qd = pidx & 3;
        const unsigned pb = WS_PART + it * PART_BYTES + (((pidx >> 2) * 16 + (lane >> 2)) * 2 + (qd >> 1)) * PART_H;
        v4u t0, t1, tm;
        for (unsigned spin = 0;; ++spin) {
          asm volatile("" ::: "memory");
          t0 = get(pb + ((qd & 1) * 32 + w * 8 + (lane & 3) * 2) * 16);
          t1 = get(pb + ((qd & 1) * 32 + w * 8 + (lane & 3) * 2 + 1) * 16);
          tm = get(pb + 64 * 16);
          if (__all(ok4(t0) && ok4(t1) && ok4(tm)) || dead) break;
          if (spin > SPIN_CAP) { dead = true; break; }
        }
        STAMP(6 + 16 * it);
        const float ms = __uint_as_float(tm.x);
        float M = fmaxf(ms, dppf<ROR4>(ms)); M = fmaxf(M, dppf<ROR8>(M)); M = xrow_max(M);
        const float f = __builtin_amdgcn_exp2f(ms - M);
        float v[5] = {f * __uint_as_float(t0.x), f * __uint_as_float(t1.x), f * __uint_as_float(t0.z), f * __uint_as_float(t1.z), f * __uint_as_float(tm.z)};
#pragma unroll
        for (int i = 0; i < 5; ++i) { v[i] += dppf<ROR4>(v[i]); v[i] += dppf<ROR8>(v[i]); v[i] = xrow_sum(v[i]); }
        const float inv = __builtin_amdgcn_rcpf(v[4]);
        const int jj = lane & 3, slot = 4 * w + ((lane >> 2) & 1) * 2 + (jj >> 1);
        if (lane < 8) *LDSP(unsigned, LDS_XO + ((slot & ~7) + (slot & 3) * 2 + ((slot >> 2) & 1)) * 8 + (jj & 1) * 4) = lane < 4 ? cvtpk(v[0] * inv, v[1] * inv) : cvtpk(v[2] * inv, v[3] * inv);
      }
      wg_barrier();
      proj_run(I2, std::integral_constant<int, PRE>{}, pj, wf, WS_YO + it * YO_BYTES);
      STAMP(7 + 16 * it);
      if (isR) {
        const v2u xo = reduce_cols(I2, WS_YO + it * YO_BYTES);
        LANES;
        if (tid < 16 && !(tid & 3)) put(WS_X + (2 * it) * X_BYTES + (ridx * 4 + (tid >> 2)) * 16, xo.x, xo.y);
        STAMP(8 + 16 * it);
      }
      wg_barrier();
      if (it == 0) load_wq();
      if (isR) {
        const v2u xn = reduce_cols(std::integral_constant<int, 6>{}, WS_YD + it * YD_BYTES);
        LANES;
        if (tid < 16 && !(tid & 3)) {
          if (it == 0) put(WS_X + X_BYTES + (ridx * 4 + (tid >> 2)) * 16, xn.x, xn.y);
          else *(v2u*)(a.out + (ridx * 16 + tid) * 2) = xn;
        }
        if (it == 1 && ridx == 0 && tid == 0) __builtin_amdgcn_raw_buffer_store_b32(opqs(tagA), rs, WS_EPOCH, 0, 16);
        STAMP(12 + 16 * it);
      }
    } else {
      load_ffn();
      vmwait<0>();
      STAMP(13 + 16 * it);
      wg_barrier();
      v4u wd[9];
      Proj pj;
      {
        LANES;
        const unsigned wa = LDS_WIN + (32 * w + 4 * g4 + (c16 >> 2)) * 96 + (lane & 3) * 8;
        v4u wf[9];
#pragma unroll
        for (int i = 0; i < 9; ++i) wf[i] = lds_frag(wa + (i / 3) * (128 * 96) + (i % 3) * 32, 16 * 96);
        v2u xr[3];
        gather_x(xr, WS_X + (2 * it) * X_BYTES, lane);
        STAMP(9 + 16 * it);
        float s1, s2;
        stage_x(xr, w, lane, s1, s2);
        const unsigned xa = (c16 < 8 ? LDS_XH + w * 1280 : LDS_ONES) + w * 64 + g4 * 16;
        v4u av[2];
#pragma unroll
        for (int i = 0; i < 2; ++i) av[i] = lds_r128(xa + i * 256);
        SB();
        float mean, inv;
        ln_stats(s1, s2, mean, inv);
        f32x4 d0 = {0, 0, 0, 0}, d1 = {0, 0, 0, 0}, d2 = {0, 0, 0, 0};
        SB();
        mfma3(d0, d1, d2, av[0], wf[0], wf[1], wf[2]);
        av[0] = lds_r128(xa + 2 * 256);
#pragma unroll
        for (int i = 0; i < 3; ++i) wf[i] = lds_frag(wa + 3 * (128 * 96) + i * 32, 16 * 96);
        SB();
        mfma3(d0, d1, d2, av[1], wf[3], wf[4], wf[5]);
        av[1] = lds_r128(xa + 3 * 256);
        const unsigned k5 = w < 2 ? 4 : 3;
#pragma unroll
        for (int i = 0; i < 3; ++i) wf[3 + i] = lds_frag(wa + k5 * (128 * 96) + i * 32, 16 * 96);
        SB();
        mfma3(d0, d1, d2, av[0], wf[6], wf[7], wf[8]);
        av[0] = lds_r128(w < 2 ? xa + 4 * 256 : LDS_ZERO);
        SB();
        mfma3(d0, d1, d2, av[1], wf[0], wf[1], wf[2]);
        mfma3(d0, d1, d2, av[0], wf[3], wf[4], wf[5]);
        pj = proj_setup(I1, w, lane);
        sfor<9>([&](auto Nc) { wd[Nc.value] = proj_frag(I1, pj, Nc); });
        SB();
        mfma_fence(d0); mfma_fence(d1); mfma_fence(d2);
        *LDSP(float, LDS_RED + (((g4 >> 1) * 48 + (g4 & 1) * 16 + c16) * 4 + w) * 4) = (g4 & 1) ? d1[0] : d0[0];
        if (!(g4 & 1)) *LDSP(float, LDS_RED + (((g4 >> 1) * 48 + 32 + c16) * 4 + w) * 4) = d2[0];
        wg_barrier();
        const int l5 = lane & 31, col = (l5 < 24 ? l5 : 0) + (lane >> 5) * 24;
        const f32x4 ps = *LDSP(f32x4, LDS_RED + col * 16), pc = *LDSP(f32x4, LDS_RED + (48 + col) * 16);
        const unsigned yb = cvtpk((((ps[0] + ps[1]) + (ps[2] + ps[3])) - mean * ((pc[0] + pc[1]) + (pc[2] + pc[3]))) * inv, 0.f) << 16;
        auto sw = __builtin_amdgcn_permlane32_swap(yb, yb, false, false);
        const float gb = __uint_as_float(sw[0]), ub = __uint_as_float(sw[1]);
        const float z2 = gb * (1.0f + 0.044715f * gb * gb) * (2.0f * 0.7978845608028654f * 1.4426950408889634f);
        const float th = 1.0f - 2.0f * __builtin_amdgcn_rcpf(__builtin_amdgcn_exp2f(z2) + 1.0f);
        const float ge = rbf(0.5f * gb * (1.0f + th));
        const unsigned hb = lane < 24 ? (cvtpk(ge * ub, 0.f) & 0xffffu) : 0u;
        const int q = lane >> 2;
        if (lane < 32) *LDSP(unsigned short, LDS_XO + w * 64 + (((q & 3) * 2 + (q >> 2)) * 4 + (lane & 3)) * 2) = (unsigned short)hb;
        lgkwait();
      }
      STAMP(10 + 16 * it);
      proj_run(I1, std::integral_constant<int, 9>{}, pj, wd, WS_YD + it * YD_BYTES);
      STAMP(11 + 16 * it);
      if (it == 0) { wg_barrier(); refill_window(); }
    }
  }
  STAMP_DUMP();
#undef LANES
}

}

extern "C" void kernel_launch(void* const* d_in, const int* in_sizes, int n_in, void* d_out, int out_size, void* d_ws, size_t ws_size,
                              hipStream_t stream) {
  Args a;
  a.x0 = (const unsigned char*)d_in[0];
  a.k = (const unsigned char*)d_in[1];
  a.v = (const unsigned char*)d_in[2];
  a.wq = (const unsigned char*)d_in[3];
  a.wo = (const unsigned char*)d_in[4];
  a.wg = (const unsigned char*)d_in[6];
  a.wu = (const unsigned char*)d_in[7];
  a.wd = (const unsigned char*)d_in[8];
  a.out = (unsigned char*)d_out;
  a.ws = (unsigned char*)d_ws;
  (void)hipFuncSetAttribute((const void*)fused_decode_kernel, hipFuncAttributeMaxDynamicSharedMemorySize, LDS_BYTES);
  hipLaunchKernelGGL(fused_decode_kernel, dim3(256), dim3(256), LDS_BYTES, stream, a);
}
```

```cpp
#include <hip/hip_runtime.h>
#include <hip/hip_bf16.h>
#include <stdint.h>
#include <utility>

#pragma clang diagnostic ignored "-Winline-asm"

namespace {

typedef unsigned v4u __attribute__((ext_vector_type(4)));
typedef unsigned v2u __attribute__((ext_vector_type(2)));
typedef float f32x4 __attribute__((ext_vector_type(4)));
typedef float f32x2 __attribute__((ext_vector_type(2)));
typedef __bf16 bf16x2 __attribute__((ext_vector_type(2)));
typedef short v4s __attribute__((ext_vector_type(4)));
typedef unsigned v16u __attribute__((ext_vector_type(16)));
typedef unsigned v8u __attribute__((ext_vector_type(8)));
#define DI __device__ __forceinline__
#define LAS __attribute__((address_space(3)))
#define ACL "a0", "a1", "a2", "a3", "a4", "a5", "a6", "a7", "a8", "a9", "a10", "a11", "a12", "a13", "a14", "a15", "a16", "a17", "a18", "a19", "a20", "a21", "a22", "a23", "a24", "a25", "a26", "a27", "a28", "a29", "a30", "a31", "a32", "a33", "a34", "a35", "a36", "a37", "a38", "a39", "a40", "a41", "a42", "a43", "a44", "a45", "a46", "a47", "a48", "a49", "a50", "a51", "a52", "a53", "a54", "a55", "a56", "a57", "a58", "a59", "a60", "a61", "a62", "a63"

constexpr int DM = 576, SK = 65536, DF = 4608;
constexpr int NPD = 61, NFD = 65;
constexpr int CAP = 64;
constexpr int NL = 4, NW = 5;
#define PACE 905
#define PACE2 915
#define LEAD 150
#define PWQ 9
#define AHEAD 1
#define AGE 0xb0
#define PSHIFT 5
#define ROT 6
#define EARLY 1
constexpr float C2 = 0.088388347648318440f * 1.4426950408889634f;
constexpr unsigned SPIN_CAP = 1u << 17;

constexpr int LDS_WIN = 0, WIN_DN = 55296, WIN_BYTES = 82944;
constexpr int LDS_RET = WIN_BYTES;
constexpr int LDS_MISC = LDS_RET + 4 * NL * 4096;
constexpr int LDS_XH = LDS_MISC;
constexpr int LDS_EXC = LDS_MISC + 1152;
constexpr int LDS_ZERO = LDS_MISC + 6272;
constexpr int LDS_CMB = LDS_MISC + 6400;
constexpr int LDS_XO = LDS_MISC + 10624;
constexpr int LDS_RED = LDS_MISC + 10880;
constexpr int LDS_ONES = LDS_MISC + 12416;
constexpr int LDS_BASE = LDS_MISC + 13568;
#define LDS_PACE_ADDR 0x27ff0
constexpr int LDS_BYTES = 163840;
static_assert(LDS_BASE + 32 <= LDS_PACE_ADDR && 4 * NW * 4096 <= WIN_BYTES, "LDS map");

constexpr unsigned WS_EPOCH = 0;
constexpr unsigned WS_Q = 4096, Q_BYTES = 256 * 128;
constexpr unsigned WS_X = WS_Q + 2 * Q_BYTES, X_BYTES = 8192;
constexpr unsigned WS_PART = WS_X + 4 * X_BYTES, PART_H = 80 * 16, PART_BYTES = 256 * 2 * PART_H;
constexpr unsigned WS_YO = WS_PART + 2 * PART_BYTES, YO_BYTES = 36 * 64 * 128;
constexpr unsigned WS_YD = WS_YO + 2 * YO_BYTES, YD_BYTES = 36 * 192 * 128;
constexpr unsigned WS_SYNC = WS_YD + 2 * YD_BYTES, SYNC_BYTES = 128 * 128;
constexpr unsigned WS_END = WS_SYNC + SYNC_BYTES;
static_assert(WS_SYNC % 128 == 0, "line aligned");

struct Args {
  const unsigned char *x0, *k, *v, *wq, *wo, *wg, *wu, *wd;
  unsigned char *out, *ws;
};

template <int... I, class F> DI void sfor_impl(std::integer_sequence<int, I...>, F&& f) { (f(std::integral_constant<int, I>{}), ...); }
template <int N> using IC = std::integral_constant<int, N>;
template <int N, class F> DI void sfor(F&& f) { sfor_impl(std::make_integer_sequence<int, N>{}, f); }

template <int CTRL> DI float dppf(float x) { return __builtin_bit_cast(float, __builtin_amdgcn_mov_dpp(__builtin_bit_cast(int, x), CTRL, 0xf, 0xf, true)); }
template <int CTRL> DI unsigned dppu(unsigned x) { return (unsigned)__builtin_amdgcn_mov_dpp((int)x, CTRL, 0xf, 0xf, true); }
constexpr int QP1 = 0x55, QP2 = 0xAA, QP3 = 0xFF, XOR1 = 0xB1, XOR2 = 0x4E, HMIR = 0x141, ROR4 = 0x124, ROR8 = 0x128;
DI unsigned cvtpk(float lo, float hi) { f32x2 v = {lo, hi}; return __builtin_bit_cast(unsigned, __builtin_convertvector(v, bf16x2)); }
DI float bflo(unsigned u) { return __builtin_bit_cast(float, u << 16); }
DI float dot2(unsigned a, unsigned b, float c) { return __builtin_amdgcn_fdot2_f32_bf16(__builtin_bit_cast(bf16x2, a), __builtin_bit_cast(bf16x2, b), c, false); }
DI float rbf(float x) { return bflo(cvtpk(x, x)); }
DI float xrow_max(float x) {
  auto s = __builtin_amdgcn_permlane16_swap(__float_as_uint(x), __float_as_uint(x), false, false);
  x = fmaxf(__uint_as_float(s[0]), __uint_as_float(s[1]));
  auto t = __builtin_amdgcn_permlane32_swap(__float_as_uint(x), __float_as_uint(x), false, false);
  return fmaxf(__uint_as_float(t[0]), __uint_as_float(t[1]));
}
DI float xrow_sum(float x) {
  auto s = __builtin_amdgcn_permlane16_swap(__float_as_uint(x), __float_as_uint(x), false, false);
  x = __uint_as_float(s[0]) + __uint_as_float(s[1]);
  auto t = __builtin_amdgcn_permlane32_swap(__float_as_uint(x), __float_as_uint(x), false, false);
  return __uint_as_float(t[0]) + __uint_as_float(t[1]);
}
DI void wg_barrier() { asm volatile("s_waitcnt lgkmcnt(0)\n\ts_barrier" ::: "memory", ACL); }
template <int N> DI void vmwait() { asm volatile("s_waitcnt vmcnt(%c0)" ::"i"(N) : "memory", ACL); }

#define LDSP(T, off) ((LAS T*)(uintptr_t)(off))
DI v4u lds_r128(unsigned a) { return *LDSP(v4u, a); }
DI v2u lds_tr(unsigned a) { return __builtin_bit_cast(v2u, __builtin_amdgcn_ds_read_tr16_b64_v4i16(LDSP(v4s, a))); }
DI v4u lds_frag(unsigned a, unsigned second) { const v2u lo = lds_tr(a), hi = lds_tr(a + second); return v4u{lo.x, lo.y, hi.x, hi.y}; }

typedef __amdgpu_buffer_rsrc_t rsrc_t;
DI rsrc_t mk_rsrc(const unsigned char* p, unsigned bytes) { return __builtin_amdgcn_make_buffer_rsrc((void*)p, 0, bytes, 0x00020000); }
DI int lane_id() { int l; asm volatile("v_mbcnt_lo_u32_b32 %0, -1, 0\n\tv_mbcnt_hi_u32_b32 %0, -1, %0" : "=v"(l)::ACL); return l; }
DI unsigned opqs(unsigned x) { asm volatile("" : "+s"(x)); return x; }

DI void glds_b(unsigned voff, rsrc_t r, unsigned soff, unsigned lds_dst) {
  unsigned keep;
  asm volatile("s_mov_b32 %0, m0\n\ts_mov_b32 m0, %4\n\ts_nop 0\n\tbuffer_load_dwordx4 %1, %2, %3 offen nt lds\n\ts_mov_b32 m0, %0"
               : "=&s"(keep) : "v"(voff), "s"(r), "s"(soff), "s"(lds_dst) : "memory");
}
DI void glds_v48(v2u vaddr, unsigned lds_dst) {
  unsigned keep;
  asm volatile("s_mov_b32 %0, m0\n\ts_mov_b32 m0, %2\n\ts_mov_b32 exec_hi, 0xffff\n\tglobal_load_lds_dwordx4 %1, off\n\ts_mov_b32 exec_hi, -1\n\ts_mov_b32 m0, %0"
               : "=&s"(keep) : "v"(vaddr), "s"(lds_dst) : "memory");
}
struct Att {
  rsrc_t k, v;
  unsigned koff, voff;
  unsigned sok, sov;
  unsigned sched, base, start, own, tau;
  unsigned st_off, ld_off, prev, pmean, tag;
  v16u words;
  unsigned clk;
  float c2;
  v8u kon;
  unsigned ck, cv, bk, bv;
  v4u q0, q1, msk;
  f32x4 acc; float m, l;
  unsigned lsc;
  v2u sc;
  unsigned lret, lwin, lexc;
  unsigned e, es;
};
#define STR2(x) #x
#define STR(x) STR2(x)
#define DPPX " row_mask:0xf bank_mask:0xf bound_ctrl:1\n\t"
DI void define_subroutines() {
  asm volatile(
      "s_branch .Lfd_end\n"
      ".Lfd_core:\n\t"
      "s_nop 5\n"
      ".Lfd_core2:\n\ts_memrealtime s[98:99]\n\t"
      "v_mov_b32_dpp v50, v28 quad_perm:[0,0,0,0]" DPPX "v_mov_b32_dpp v51, v30 quad_perm:[2,2,2,2]" DPPX
      "v_add_f32_dpp v50, v29, v50 quad_perm:[1,1,1,1]" DPPX "v_add_f32_dpp v51, v31, v51 quad_perm:[3,3,3,3]" DPPX
      "v_add_f32_e32 v50, v50, v51\n"
      ".Lfd_core3:\n\tv_fma_f32 v51, v50, s82, -v48\n\tv_cmp_lt_f32_e32 vcc, 0, v51\n\ts_cbranch_vccnz .Lfd_newmax\n\t"
      "v_exp_f32_e32 v50, v51\n\ts_nop 0\n\tv_add_f32_e32 v49, v49, v50\n"
      ".Lfd_join:\n\tv_cvt_pk_bf16_f32 v50, v50, v50\n\t"
      "v_and_b32_e32 v54, v40, v50\n\tv_and_b32_e32 v55, v41, v50\n\tv_and_b32_e32 v60, v42, v50\n\tv_and_b32_e32 v61, v43, v50\n\t"
      "v_and_b32_dpp v56, v50, v42 row_shr:4" DPPX "v_and_b32_dpp v57, v50, v43 row_shr:4" DPPX
      "v_and_b32_dpp v58, v50, v40 row_shl:4" DPPX "v_and_b32_dpp v59, v50, v41 row_shl:4" DPPX
      "s_setpc_b64 s[94:95]\n"
      ".Lfd_newmax:\n\t"
      "v_max_f32_dpp v51, v50, v50 row_half_mirror" DPPX
      "v_mov_b32_e32 v52, v51\n\ts_nop 1\n\tv_permlane16_swap_b32_e32 v51, v52\n\tv_max_f32_e32 v51, v51, v52\n\t"
      "v_mov_b32_e32 v52, v51\n\ts_nop 1\n\tv_permlane32_swap_b32_e32 v51, v52\n\tv_max_f32_e32 v51, v51, v52\n\t"
      "v_mul_f32_e32 v51, s82, v51\n\tv_max_f32_e32 v51, v48, v51\n\t"
      "v_sub_f32_e32 v52, v48, v51\n\tv_fma_f32 v50, v50, s82, -v51\n\t"
      "v_exp_f32_e32 v52, v52\n\tv_exp_f32_e32 v50, v50\n\tv_mov_b32_e32 v48, v51\n\t"
      "v_pk_mul_f32 v[44:45], v[44:45], v[52:53] op_sel_hi:[1,0]\n\tv_pk_mul_f32 v[46:47], v[46:47], v[52:53] op_sel_hi:[1,0]\n\t"
      "v_fma_f32 v49, v49, v52, v50\n\ts_branch .Lfd_join\n"
      ".Lfd_kred:\n\ts_memrealtime s[98:99]\n\ts_nop 4\n\t"
      "v_mov_b32_dpp v50, v28 quad_perm:[0,0,0,0]" DPPX "v_mov_b32_dpp v52, v30 quad_perm:[2,2,2,2]" DPPX
      "v_add_f32_dpp v50, v29, v50 quad_perm:[1,1,1,1]" DPPX "v_add_f32_dpp v52, v31, v52 quad_perm:[3,3,3,3]" DPPX "v_add_f32_e32 v50, v50, v52\n\t"
      "v_mov_b32_dpp v51, v54 quad_perm:[0,0,0,0]" DPPX "v_mov_b32_dpp v52, v56 quad_perm:[2,2,2,2]" DPPX
      "v_add_f32_dpp v51, v55, v51 quad_perm:[1,1,1,1]" DPPX "v_add_f32_dpp v52, v57, v52 quad_perm:[3,3,3,3]" DPPX "v_add_f32_e32 v51, v51, v52\n\t"
      "s_setpc_b64 s[94:95]\n"
      ".Lfd_pace0:\n\t"
      "v_mov_b32_e32 v52, " STR(LDS_PACE_ADDR) "\n\ts_cmp_lt_i32 s80, 0\n\ts_cbranch_scc1 .Lfd_p1\n\t"
      "s_waitcnt lgkmcnt(0)\n\ts_mov_b32 s87, 0\n\t"
      "s_cmp_eq_u32 s57, s74\n\ts_cbranch_scc0 .Lfd_p1\n\ts_sub_u32 s92, s56, s90\n\ts_add_u32 s87, s87, s92\n\t"
      "s_cmp_eq_u32 s59, s74\n\ts_cbranch_scc0 .Lfd_p1\n\ts_sub_u32 s92, s58, s90\n\ts_add_u32 s87, s87, s92\n\t"
      "s_cmp_eq_u32 s61, s74\n\ts_cbranch_scc0 .Lfd_p1\n\ts_sub_u32 s92, s60, s90\n\ts_add_u32 s87, s87, s92\n\t"
      "s_cmp_eq_u32 s63, s74\n\ts_cbranch_scc0 .Lfd_p1\n\ts_sub_u32 s92, s62, s90\n\ts_add_u32 s87, s87, s92\n\t"
      "s_cmp_eq_u32 s65, s74\n\ts_cbranch_scc0 .Lfd_p1\n\ts_sub_u32 s92, s64, s90\n\ts_add_u32 s87, s87, s92\n\t"
      "s_cmp_eq_u32 s67, s74\n\ts_cbranch_scc0 .Lfd_p1\n\ts_sub_u32 s92, s66, s90\n\ts_add_u32 s87, s87, s92\n\t"
      "s_cmp_eq_u32 s69, s74\n\ts_cbranch_scc0 .Lfd_p1\n\ts_sub_u32 s92, s68, s90\n\ts_add_u32 s87, s87, s92\n\t"
      "s_cmp_eq_u32 s71, s74\n\ts_cbranch_scc0 .Lfd_p1\n\ts_sub_u32 s92, s70, s90\n\ts_add_u32 s87, s87, s92\n\t"
      "s_cmp_eq_u32 s85, 0\n\ts_cselect_b32 s85, s87, s85\n\ts_add_u32 s92, s87, s85\n\ts_mov_b32 s85, s87\n\ts_ashr_i32 s92, s92, 4\n\t"
      "s_sub_i32 s87, s92, s54\n\ts_mov_b32 s54, s92\n\ts_mul_i32 s87, s87, " STR(AHEAD) "\n\ts_ashr_i32 s87, s87, 1\n\t"
      "s_add_u32 s92, s92, s87\n\ts_add_u32 s92, s92, s90\n\ts_not_b32 s57, s74\n\t"
      "v_mov_b32_e32 v50, s92\n\tv_mov_b32_e32 v51, s74\n\tds_write_b64 v52, v[50:51]\n"
      ".Lfd_p1:\n\t"
      "ds_read_b64 v[50:51], v52\n\ts_waitcnt lgkmcnt(0)\n\tv_readfirstlane_b32 s87, v50\n\tv_readfirstlane_b32 s92, v51\n\t"
      "s_sub_u32 s87, s87, s90\n\ts_sub_i32 s87, s87, " STR(LEAD) "\n\ts_max_i32 s87, s87, 0\n\ts_cmp_eq_u32 s92, s74\n\ts_cselect_b32 s87, s87, 0\n\t"
      "s_add_u32 s89, s90, s87\n"
      ".Lfd_pace:\n\t"
      "s_add_u32 s88, s88, s84\n\ts_movk_i32 s87, 48\n\t"
      "s_lshl_b32 s92, s98, 4\n\ts_sub_u32 s91, s92, s88\n\ts_sub_u32 s93, s91, s89\n\ts_cmp_ge_i32 s93, 0\n\ts_cbranch_scc1 .Lfd_pd\n"
      ".Lfd_pl:\n\t"
      "s_memrealtime s[92:93]\n\ts_waitcnt lgkmcnt(0)\n\ts_lshl_b32 s92, s92, 4\n\ts_sub_u32 s91, s92, s88\n\ts_sub_u32 s93, s91, s89\n\t"
      "s_cmp_ge_i32 s93, 0\n\ts_cbranch_scc1 .Lfd_pd\n\ts_sleep 1\n\ts_sub_i32 s87, s87, 1\n\ts_cmp_gt_i32 s87, 0\n\ts_cbranch_scc1 .Lfd_pl\n"
      ".Lfd_pd:\n\ts_sub_u32 s91, s91, s90\n\ts_min_i32 s55, s55, s91\n"
      ".Lfd_ret:\n\ts_setpc_b64 s[94:95]\n"
      ".Lfd_sync:\n\t"
      "s_cmp_lt_i32 s81, 0\n\ts_cbranch_scc1 .Lfd_ret\n\t"
      "s_lshr_b32 s92, s92, 5\n\ts_and_b32 s92, s92, 0x3f80\n\t"
      "s_add_u32 s91, s55, s90\n\ts_brev_b32 s55, -2\n\tv_mov_b32_e32 v50, s91\n\tv_mov_b32_e32 v51, s74\n\ts_add_u32 s92, s92, s81\n\t"
      "s_mov_b64 exec, 1\n\tbuffer_store_dwordx2 v[50:51], off, s[76:79], s92 sc1\n\ts_mov_b64 exec, -1\n\ts_setpc_b64 s[94:95]\n"
      ".Lfd_hear:\n\t"
      "s_cmp_lt_i32 s80, 0\n\ts_cbranch_scc1 .Lfd_ret\n\t"
      "s_lshr_b32 s92, s98, 1\n\ts_sub_u32 s93, s92, " STR(AGE) "\n\ts_and_b32 s93, s93, 0x3f80\n\t"
      "s_xor_b32 s80, s80, 64\n\ts_add_u32 s93, s93, s80\n\ts_buffer_load_dwordx16 s[56:71], s[76:79], s93\n\ts_setpc_b64 s[94:95]\n"
      ".macro FD_PACK src\n\t"
      "v_perm_b32 v62, v[\\src+1], v[\\src+0], s45\n\tv_perm_b32 v63, v[\\src+3], v[\\src+2], s45\n\tv_perm_b32 v8, v[\\src+1], v[\\src+0], s44\n\tv_perm_b32 v9, v[\\src+3], v[\\src+2], s44\n\t"
      "v_and_b32_e32 v10, 0x7f7f7f7f, v62\n\tv_and_b32_e32 v50, 0x7f7f7f7f, v63\n\tv_and_b32_e32 v20, 0x7f7f7f7f, v8\n\tv_add_u32_e32 v21, 0x7f7f7f7f, v10\n\t"
      "v_add_u32_e32 v20, 0x7f7f7f7f, v20\n\tv_xor_b32_e32 v51, s75, v10\n\tv_bitop3_b32 v21, v20, v8, v21 bitop3:0x01\n\tv_add_u32_e32 v51, 0x7f7f7f7f, v51\n\t"
      "v_and_b32_e32 v21, 0x80808080, v21\n\tv_bitop3_b32 v51, v20, v8, v51 bitop3:0x01\n\tv_lshrrev_b32_e32 v20, 7, v21\n\tv_sub_u32_e32 v21, v21, v20\n\t"
      "v_bfi_b32 v10, v21, s75, v10\n\tv_and_b32_e32 v22, 0x7f7f7f7f, v9\n\tv_add_u32_e32 v23, 0x7f7f7f7f, v50\n\tv_add_u32_e32 v22, 0x7f7f7f7f, v22\n\t"
      "v_xor_b32_e32 v52, s75, v50\n\tv_bitop3_b32 v23, v22, v9, v23 bitop3:0x01\n\tv_add_u32_e32 v52, 0x7f7f7f7f, v52\n\tv_and_b32_e32 v23, 0x80808080, v23\n\t"
      "v_bitop3_b32 v52, v22, v9, v52 bitop3:0x01\n\tv_lshrrev_b32_e32 v22, 7, v23\n\tv_sub_u32_e32 v23, v23, v22\n\tv_bfi_b32 v50, v23, s75, v50\n\t"
      "v_add_u32_e32 v10, s72, v10\n\tv_add_u32_e32 v50, s72, v50\n\tv_or_b32_e32 v51, v51, v52\n\tv_bitop3_b32 v52, v10, v50, s48 bitop3:0x7e\n\t"
      "v_and_b32_e32 v51, 0x80808080, v51\n\tv_and_or_b32 v51, v52, s51, v51\n\tv_cmp_ne_u32_e32 vcc, 0, v51\n\tv_lshrrev_b32_e32 v62, 4, v62\n\t"
      "v_lshlrev_b32_e32 v50, 4, v50\n\tv_bfi_b32 v10, s49, v10, v62\n\tv_addc_co_u32_e32 v7, vcc, v7, v7, vcc\n\tv_bfi_b32 v50, s48, v63, v50\n\t"
      "v_bfi_b32 v10, s50, v10, v50\n\tv_perm_b32 v62, v[\\src+5], v[\\src+4], s45\n\tv_perm_b32 v63, v[\\src+7], v[\\src+6], s45\n\tv_perm_b32 v11, v[\\src+5], v[\\src+4], s44\n\t"
      "v_perm_b32 v12, v[\\src+7], v[\\src+6], s44\n\tv_and_b32_e32 v13, 0x7f7f7f7f, v62\n\tv_and_b32_e32 v50, 0x7f7f7f7f, v63\n\tv_and_b32_e32 v20, 0x7f7f7f7f, v11\n\t"
      "v_add_u32_e32 v21, 0x7f7f7f7f, v13\n\tv_add_u32_e32 v20, 0x7f7f7f7f, v20\n\tv_xor_b32_e32 v51, s75, v13\n\tv_bitop3_b32 v21, v20, v11, v21 bitop3:0x01\n\t"
      "v_add_u32_e32 v51, 0x7f7f7f7f, v51\n\tv_and_b32_e32 v21, 0x80808080, v21\n\tv_bitop3_b32 v51, v20, v11, v51 bitop3:0x01\n\tv_lshrrev_b32_e32 v20, 7, v21\n\t"
      "v_sub_u32_e32 v21, v21, v20\n\tv_bfi_b32 v13, v21, s75, v13\n\tv_and_b32_e32 v22, 0x7f7f7f7f, v12\n\tv_add_u32_e32 v23, 0x7f7f7f7f, v50\n\t"
      "v_add_u32_e32 v22, 0x7f7f7f7f, v22\n\tv_xor_b32_e32 v52, s75, v50\n\tv_bitop3_b32 v23, v22, v12, v23 bitop3:0x01\n\tv_add_u32_e32 v52, 0x7f7f7f7f, v52\n\t"
      "v_and_b32_e32 v23, 0x80808080, v23\n\tv_bitop3_b32 v52, v22, v12, v52 bitop3:0x01\n\tv_lshrrev_b32_e32 v22, 7, v23\n\tv_sub_u32_e32 v23, v23, v22\n\t"
      "v_bfi_b32 v50, v23, s75, v50\n\tv_add_u32_e32 v13, s72, v13\n\tv_add_u32_e32 v50, s72, v50\n\tv_or_b32_e32 v51, v51, v52\n\t"
      "v_bitop3_b32 v52, v13, v50, s48 bitop3:0x7e\n\tv_and_b32_e32 v51, 0x80808080, v51\n\tv_and_or_b32 v51, v52, s51, v51\n\tv_cmp_ne_u32_e32 vcc, 0, v51\n\t"
      "v_lshrrev_b32_e32 v62, 4, v62\n\tv_lshlrev_b32_e32 v50, 4, v50\n\tv_bfi_b32 v13, s49, v13, v62\n\tv_addc_co_u32_e32 v7, vcc, v7, v7, vcc\n\t"
      "v_bfi_b32 v50, s48, v63, v50\n\tv_bfi_b32 v13, s50, v13, v50\n\tv_perm_b32 v62, v[\\src+9], v[\\src+8], s45\n\tv_perm_b32 v63, v[\\src+11], v[\\src+10], s45\n\t"
      "v_perm_b32 v14, v[\\src+9], v[\\src+8], s44\n\tv_perm_b32 v15, v[\\src+11], v[\\src+10], s44\n\tv_and_b32_e32 v16, 0x7f7f7f7f, v62\n\tv_and_b32_e32 v50, 0x7f7f7f7f, v63\n\t"
      "v_and_b32_e32 v20, 0x7f7f7f7f, v14\n\tv_add_u32_e32 v21, 0x7f7f7f7f, v16\n\tv_add_u32_e32 v20, 0x7f7f7f7f, v20\n\tv_xor_b32_e32 v51, s75, v16\n\t"
      "v_bitop3_b32 v21, v20, v14, v21 bitop3:0x01\n\tv_add_u32_e32 v51, 0x7f7f7f7f, v51\n\tv_and_b32_e32 v21, 0x80808080, v21\n\tv_bitop3_b32 v51, v20, v14, v51 bitop3:0x01\n\t"
      "v_lshrrev_b32_e32 v20, 7, v21\n\tv_sub_u32_e32 v21, v21, v20\n\tv_bfi_b32 v16, v21, s75, v16\n\tv_and_b32_e32 v22, 0x7f7f7f7f, v15\n\t"
      "v_add_u32_e32 v23, 0x7f7f7f7f, v50\n\tv_add_u32_e32 v22, 0x7f7f7f7f, v22\n\tv_xor_b32_e32 v52, s75, v50\n\tv_bitop3_b32 v23, v22, v15, v23 bitop3:0x01\n\t"
      "v_add_u32_e32 v52, 0x7f7f7f7f, v52\n\tv_and_b32_e32 v23, 0x80808080, v23\n\tv_bitop3_b32 v52, v22, v15, v52 bitop3:0x01\n\tv_lshrrev_b32_e32 v22, 7, v23\n\t"
      "v_sub_u32_e32 v23, v23, v22\n\tv_bfi_b32 v50, v23, s75, v50\n\tv_add_u32_e32 v16, s72, v16\n\tv_add_u32_e32 v50, s72, v50\n\t"
      "v_or_b32_e32 v51, v51, v52\n\tv_bitop3_b32 v52, v16, v50, s48 bitop3:0x7e\n\tv_and_b32_e32 v51, 0x80808080, v51\n\tv_and_or_b32 v51, v52, s51, v51\n\t"
      "v_cmp_ne_u32_e32 vcc, 0, v51\n\tv_lshrrev_b32_e32 v62, 4, v62\n\tv_lshlrev_b32_e32 v50, 4, v50\n\tv_bfi_b32 v16, s49, v16, v62\n\t"
      "v_addc_co_u32_e32 v7, vcc, v7, v7, vcc\n\tv_bfi_b32 v50, s48, v63, v50\n\tv_bfi_b32 v16, s50, v16, v50\n\tv_perm_b32 v62, v[\\src+13], v[\\src+12], s45\n\t"
      "v_perm_b32 v63, v[\\src+15], v[\\src+14], s45\n\tv_perm_b32 v17, v[\\src+13], v[\\src+12], s44\n\tv_perm_b32 v18, v[\\src+15], v[\\src+14], s44\n\tv_and_b32_e32 v19, 0x7f7f7f7f, v62\n\t"
      "v_and_b32_e32 v50, 0x7f7f7f7f, v63\n\tv_and_b32_e32 v20, 0x7f7f7f7f, v17\n\tv_add_u32_e32 v21, 0x7f7f7f7f, v19\n\tv_add_u32_e32 v20, 0x7f7f7f7f, v20\n\t"
      "v_xor_b32_e32 v51, s75, v19\n\tv_bitop3_b32 v21, v20, v17, v21 bitop3:0x01\n\tv_add_u32_e32 v51, 0x7f7f7f7f, v51\n\tv_and_b32_e32 v21, 0x80808080, v21\n\t"
      "v_bitop3_b32 v51, v20, v17, v51 bitop3:0x01\n\tv_lshrrev_b32_e32 v20, 7, v21\n\tv_sub_u32_e32 v21, v21, v20\n\tv_bfi_b32 v19, v21, s75, v19\n\t"
      "v_and_b32_e32 v22, 0x7f7f7f7f, v18\n\tv_add_u32_e32 v23, 0x7f7f7f7f, v50\n\tv_add_u32_e32 v22, 0x7f7f7f7f, v22\n\tv_xor_b32_e32 v52, s75, v50\n\t"
      "v_bitop3_b32 v23, v22, v18, v23 bitop3:0x01\n\tv_add_u32_e32 v52, 0x7f7f7f7f, v52\n\tv_and_b32_e32 v23, 0x80808080, v23\n\tv_bitop3_b32 v52, v22, v18, v52 bitop3:0x01\n\t"
      "v_lshrrev_b32_e32 v22, 7, v23\n\tv_sub_u32_e32 v23, v23, v22\n\tv_bfi_b32 v50, v23, s75, v50\n\tv_add_u32_e32 v19, s72, v19\n\t"
      "v_add_u32_e32 v50, s72, v50\n\tv_or_b32_e32 v51, v51, v52\n\tv_bitop3_b32 v52, v19, v50, s48 bitop3:0x7e\n\tv_and_b32_e32 v51, 0x80808080, v51\n\t"
      "v_and_or_b32 v51, v52, s51, v51\n\tv_cmp_ne_u32_e32 vcc, 0, v51\n\tv_lshrrev_b32_e32 v62, 4, v62\n\tv_lshlrev_b32_e32 v50, 4, v50\n\t"
      "v_bfi_b32 v19, s49, v19, v62\n\tv_addc_co_u32_e32 v7, vcc, v7, v7, vcc\n\tv_bfi_b32 v50, s48, v63, v50\n\tv_bfi_b32 v19, s50, v19, v50\n\t"
      "s_setpc_b64 s[94:95]\n.endm\n"
      ".Lfd_pack0:\n\tFD_PACK 64\n"
      ".Lfd_pack1:\n\tFD_PACK 80\n"
      ".Lfd_pack2:\n\tFD_PACK 96\n"
      ".Lfd_pack3:\n\tFD_PACK 112\n"
      ".purgem FD_PACK\n"
      ".Lfd_uproc:\n\tv_add_co_u32_e32 v7, vcc, v7, v7\n\tv_and_b32_e32 v62, 0x7070707, v10\n\tv_lshrrev_b32_e32 v63, 4, v10\n\t"
      "v_and_or_b32 v20, v8, s49, v62\n\tv_and_b32_e32 v63, 0x7070707, v63\n\tv_add_u32_e32 v20, 0x7f7f7f7f, v20\n\tv_and_or_b32 v21, v9, s49, v63\n\t"
      "v_bitop3_b32 v20, v20, v8, s48 bitop3:0xa8\n\tv_add_u32_e32 v21, 0x7f7f7f7f, v21\n\tv_lshrrev_b32_e32 v22, 7, v20\n\tv_bitop3_b32 v21, v21, v9, s48 bitop3:0xa8\n\t"
      "v_sub_u32_e32 v20, v20, v22\n\tv_lshrrev_b32_e32 v22, 7, v21\n\tv_and_b32_e32 v20, s75, v20\n\tv_sub_u32_e32 v21, v21, v22\n\t"
      "v_add_u32_e32 v62, v20, v62\n\tv_and_b32_e32 v21, s75, v21\n\tv_lshlrev_b32_e32 v23, 4, v10\n\tv_add_u32_e32 v63, v21, v63\n\t"
      "v_bfi_b32 v62, s48, v23, v62\n\tv_bfi_b32 v63, s48, v10, v63\n\tv_perm_b32 v20, v62, v8, s46\n\tv_perm_b32 v21, v62, v8, s47\n\t"
      "v_perm_b32 v22, v63, v9, s46\n\tv_perm_b32 v23, v63, v9, s47\n\ts_cbranch_vccnz .Lfd_f0\n\t.Lfd_u0:\n\t"
      "v_add_co_u32_e32 v7, vcc, v7, v7\n\tv_and_b32_e32 v62, 0x7070707, v13\n\tv_lshrrev_b32_e32 v63, 4, v13\n\tv_and_or_b32 v24, v11, s49, v62\n\t"
      "v_and_b32_e32 v63, 0x7070707, v63\n\tv_add_u32_e32 v24, 0x7f7f7f7f, v24\n\tv_and_or_b32 v25, v12, s49, v63\n\tv_bitop3_b32 v24, v24, v11, s48 bitop3:0xa8\n\t"
      "v_add_u32_e32 v25, 0x7f7f7f7f, v25\n\tv_lshrrev_b32_e32 v26, 7, v24\n\tv_bitop3_b32 v25, v25, v12, s48 bitop3:0xa8\n\tv_sub_u32_e32 v24, v24, v26\n\t"
      "v_lshrrev_b32_e32 v26, 7, v25\n\tv_and_b32_e32 v24, s75, v24\n\tv_sub_u32_e32 v25, v25, v26\n\tv_add_u32_e32 v62, v24, v62\n\t"
      "v_and_b32_e32 v25, s75, v25\n\tv_lshlrev_b32_e32 v27, 4, v13\n\tv_add_u32_e32 v63, v25, v63\n\tv_bfi_b32 v62, s48, v27, v62\n\t"
      "v_bfi_b32 v63, s48, v13, v63\n\tv_perm_b32 v24, v62, v11, s46\n\tv_perm_b32 v25, v62, v11, s47\n\tv_perm_b32 v26, v63, v12, s46\n\t"
      "v_perm_b32 v27, v63, v12, s47\n\ts_cbranch_vccnz .Lfd_f1\n\t.Lfd_u1:\n\ts_nop 0\n\t"
      "v_mfma_f32_16x16x32_bf16 v[28:31], v[20:23], v[32:35], 0\n\tv_mfma_f32_16x16x32_bf16 v[28:31], v[24:27], v[36:39], v[28:31]\n\tv_add_co_u32_e32 v7, vcc, v7, v7\n\tv_and_b32_e32 v62, 0x7070707, v16\n\t"
      "v_lshrrev_b32_e32 v63, 4, v16\n\tv_and_or_b32 v20, v14, s49, v62\n\tv_and_b32_e32 v63, 0x7070707, v63\n\tv_add_u32_e32 v20, 0x7f7f7f7f, v20\n\t"
      "v_and_or_b32 v21, v15, s49, v63\n\tv_bitop3_b32 v20, v20, v14, s48 bitop3:0xa8\n\tv_add_u32_e32 v21, 0x7f7f7f7f, v21\n\tv_lshrrev_b32_e32 v22, 7, v20\n\t"
      "v_bitop3_b32 v21, v21, v15, s48 bitop3:0xa8\n\tv_sub_u32_e32 v20, v20, v22\n\tv_lshrrev_b32_e32 v22, 7, v21\n\tv_and_b32_e32 v20, s83, v20\n\t"
      "v_sub_u32_e32 v21, v21, v22\n\tv_add_u32_e32 v62, v20, v62\n\tv_and_b32_e32 v21, s83, v21\n\tv_lshlrev_b32_e32 v23, 4, v16\n\t"
      "v_add_u32_e32 v63, v21, v63\n\tv_bfi_b32 v62, s48, v23, v62\n\tv_bfi_b32 v63, s48, v16, v63\n\tv_perm_b32 v20, v62, v14, s46\n\t"
      "v_perm_b32 v21, v62, v14, s47\n\tv_perm_b32 v22, v63, v15, s46\n\tv_perm_b32 v23, v63, v15, s47\n\ts_cbranch_vccnz .Lfd_f2\n\t"
      ".Lfd_u2:\n\tv_add_co_u32_e32 v7, vcc, v7, v7\n\tv_and_b32_e32 v62, 0x7070707, v19\n\tv_lshrrev_b32_e32 v63, 4, v19\n\t"
      "v_and_or_b32 v24, v17, s49, v62\n\tv_and_b32_e32 v63, 0x7070707, v63\n\tv_add_u32_e32 v24, 0x7f7f7f7f, v24\n\tv_and_or_b32 v25, v18, s49, v63\n\t"
      "v_bitop3_b32 v24, v24, v17, s48 bitop3:0xa8\n\tv_add_u32_e32 v25, 0x7f7f7f7f, v25\n\tv_lshrrev_b32_e32 v26, 7, v24\n\tv_bitop3_b32 v25, v25, v18, s48 bitop3:0xa8\n\t"
      "v_sub_u32_e32 v24, v24, v26\n\tv_lshrrev_b32_e32 v26, 7, v25\n\tv_and_b32_e32 v24, s83, v24\n\tv_sub_u32_e32 v25, v25, v26\n\t"
      "v_add_u32_e32 v62, v24, v62\n\tv_and_b32_e32 v25, s83, v25\n\tv_lshlrev_b32_e32 v27, 4, v19\n\tv_add_u32_e32 v63, v25, v63\n\t"
      "v_bfi_b32 v62, s48, v27, v62\n\tv_bfi_b32 v63, s48, v19, v63\n\tv_perm_b32 v24, v62, v17, s46\n\tv_perm_b32 v25, v62, v17, s47\n\t"
      "v_perm_b32 v26, v63, v18, s46\n\tv_perm_b32 v27, v63, v18, s47\n\ts_cbranch_vccnz .Lfd_f3\n\t.Lfd_u3:\n\t"
      "s_mov_b64 s[96:97], s[94:95]\n\ts_call_b64 s[94:95], .Lfd_core2\n\tv_mfma_f32_16x16x32_bf16 v[44:47], v[20:23], v[54:57], v[44:47]\n\tv_mfma_f32_16x16x32_bf16 v[44:47], v[24:27], v[58:61], v[44:47]\n\t"
      "s_nop 5\n\ts_waitcnt lgkmcnt(0)\n\ts_setpc_b64 s[96:97]\n\t"
      ".Lfd_f0:\n\ts_mov_b64 exec, vcc\n\tds_read_b128 v[20:23], v52 offset:0\n\ts_mov_b64 exec, -1\n\ts_waitcnt lgkmcnt(0)\n\ts_branch .Lfd_u0\n\t"
      ".Lfd_f1:\n\ts_mov_b64 exec, vcc\n\tds_read_b128 v[24:27], v52 offset:1024\n\ts_mov_b64 exec, -1\n\ts_waitcnt lgkmcnt(0)\n\ts_branch .Lfd_u1\n\t"
      ".Lfd_f2:\n\ts_mov_b64 exec, vcc\n\tds_read_b128 v[20:23], v52 offset:2048\n\ts_mov_b64 exec, -1\n\ts_waitcnt lgkmcnt(0)\n\ts_branch .Lfd_u2\n\t"
      ".Lfd_f3:\n\ts_mov_b64 exec, vcc\n\tds_read_b128 v[24:27], v52 offset:3072\n\ts_mov_b64 exec, -1\n\ts_waitcnt lgkmcnt(0)\n\ts_branch .Lfd_u3\n\t"
      ".Lfd_uvproc:\n\tv_lshlrev_b32_e32 v7, 2, v7\n\tv_add_co_u32_e32 v7, vcc, v7, v7\n\tv_and_b32_e32 v62, 0x7070707, v16\n\t"
      "v_lshrrev_b32_e32 v63, 4, v16\n\tv_and_or_b32 v20, v14, s49, v62\n\tv_and_b32_e32 v63, 0x7070707, v63\n\tv_add_u32_e32 v20, 0x7f7f7f7f, v20\n\t"
      "v_and_or_b32 v21, v15, s49, v63\n\tv_bitop3_b32 v20, v20, v14, s48 bitop3:0xa8\n\tv_add_u32_e32 v21, 0x7f7f7f7f, v21\n\tv_lshrrev_b32_e32 v22, 7, v20\n\t"
      "v_bitop3_b32 v21, v21, v15, s48 bitop3:0xa8\n\tv_sub_u32_e32 v20, v20, v22\n\tv_lshrrev_b32_e32 v22, 7, v21\n\tv_and_b32_e32 v20, s83, v20\n\t"
      "v_sub_u32_e32 v21, v21, v22\n\tv_add_u32_e32 v62, v20, v62\n\tv_and_b32_e32 v21, s83, v21\n\tv_lshlrev_b32_e32 v23, 4, v16\n\t"
      "v_add_u32_e32 v63, v21, v63\n\tv_bfi_b32 v62, s48, v23, v62\n\tv_bfi_b32 v63, s48, v16, v63\n\tv_perm_b32 v20, v62, v14, s46\n\t"
      "v_perm_b32 v21, v62, v14, s47\n\tv_perm_b32 v22, v63, v15, s46\n\tv_perm_b32 v23, v63, v15, s47\n\ts_cbranch_vccnz .Lfd_f4\n\t"
      ".Lfd_u4:\n\tv_add_co_u32_e32 v7, vcc, v7, v7\n\tv_and_b32_e32 v62, 0x7070707, v19\n\tv_lshrrev_b32_e32 v63, 4, v19\n\t"
      "v_and_or_b32 v24, v17, s49, v62\n\tv_and_b32_e32 v63, 0x7070707, v63\n\tv_add_u32_e32 v24, 0x7f7f7f7f, v24\n\tv_and_or_b32 v25, v18, s49, v63\n\t"
      "v_bitop3_b32 v24, v24, v17, s48 bitop3:0xa8\n\tv_add_u32_e32 v25, 0x7f7f7f7f, v25\n\tv_lshrrev_b32_e32 v26, 7, v24\n\tv_bitop3_b32 v25, v25, v18, s48 bitop3:0xa8\n\t"
      "v_sub_u32_e32 v24, v24, v26\n\tv_lshrrev_b32_e32 v26, 7, v25\n\tv_and_b32_e32 v24, s83, v24\n\tv_sub_u32_e32 v25, v25, v26\n\t"
      "v_add_u32_e32 v62, v24, v62\n\tv_and_b32_e32 v25, s83, v25\n\tv_lshlrev_b32_e32 v27, 4, v19\n\tv_add_u32_e32 v63, v25, v63\n\t"
      "v_bfi_b32 v62, s48, v27, v62\n\tv_bfi_b32 v63, s48, v19, v63\n\tv_perm_b32 v24, v62, v17, s46\n\tv_perm_b32 v25, v62, v17, s47\n\t"
      "v_perm_b32 v26, v63, v18, s46\n\tv_perm_b32 v27, v63, v18, s47\n\ts_cbranch_vccnz .Lfd_f5\n\t.Lfd_u5:\n\t"
      "s_mov_b64 s[96:97], s[94:95]\n\ts_call_b64 s[94:95], .Lfd_core3\n\tv_mfma_f32_16x16x32_bf16 v[44:47], v[20:23], v[54:57], v[44:47]\n\tv_mfma_f32_16x16x32_bf16 v[44:47], v[24:27], v[58:61], v[44:47]\n\t"
      "s_nop 5\n\ts_waitcnt lgkmcnt(0)\n\ts_setpc_b64 s[96:97]\n\t.Lfd_f4:\n\t"
      "s_mov_b64 exec, vcc\n\tds_read_b128 v[20:23], v52 offset:2048\n\ts_mov_b64 exec, -1\n\ts_waitcnt lgkmcnt(0)\n\t"
      "s_branch .Lfd_u4\n\t.Lfd_f5:\n\ts_mov_b64 exec, vcc\n\tds_read_b128 v[24:27], v52 offset:3072\n\t"
      "s_mov_b64 exec, -1\n\ts_waitcnt lgkmcnt(0)\n\ts_branch .Lfd_u5\n\t"
      ".Lfd_scan:\n\ts_mov_b32 s87, m0\n\tv_add_co_u32_e32 v53, vcc, v53, v53\n\ts_cbranch_vccz .Lfd_s0\n\ts_add_u32 m0, s96, 0\n\ts_mov_b64 exec, vcc\n\tbuffer_load_dwordx4 v4, s[36:39], s97 offen offset:0 nt lds\n\t"
      "s_mov_b64 exec, -1\n\t.Lfd_s0:\n\tv_add_co_u32_e32 v53, vcc, v53, v53\n\ts_cbranch_vccz .Lfd_s1\n\ts_add_u32 m0, s96, 0\n\ts_mov_b64 exec, vcc\n\tbuffer_load_dwordx4 v4, s[36:39], s97 offen offset:1024 nt lds\n\t"
      "s_mov_b64 exec, -1\n\t.Lfd_s1:\n\tv_add_co_u32_e32 v53, vcc, v53, v53\n\ts_cbranch_vccz .Lfd_s2\n\ts_add_u32 m0, s96, 2048\n\ts_mov_b64 exec, vcc\n\tbuffer_load_dwordx4 v5, s[40:43], s97 offen offset:0 nt lds\n\t"
      "s_mov_b64 exec, -1\n\t.Lfd_s2:\n\tv_add_co_u32_e32 v53, vcc, v53, v53\n\ts_cbranch_vccz .Lfd_s3\n\ts_add_u32 m0, s96, 2048\n\ts_mov_b64 exec, vcc\n\tbuffer_load_dwordx4 v5, s[40:43], s97 offen offset:1024 nt lds\n\t"
      "s_mov_b64 exec, -1\n\t.Lfd_s3:\n\ts_mov_b32 m0, s87\n\tv_lshrrev_b32_e32 v53, 4, v53\n\ts_setpc_b64 s[94:95]\n\t"
      ".Lfd_end:" ::: "memory");
}
#define CODE_KB 60
DI void prefetch_code(unsigned w) {
  if (CODE_KB == 0) return;
  uint64_t pc; unsigned back;
  asm volatile("s_getpc_b64 %0\n.Lfd_pc:\n\ts_mov_b32 %1, .Lfd_pc-.Lfd_core" : "=s"(pc), "=s"(back));
  const rsrc_t r = mk_rsrc((const unsigned char*)(pc - back), CODE_KB * 1024);
  asm volatile("buffer_load_dword v80, %0, %1, 0 offen\n\tbuffer_load_dword v80, %0, %1, %2 offen" ::"v"((w * 64 + lane_id()) * 128), "s"(r), "s"(32768) : "memory");
}
#define KVIN "{v4}"(s.koff), "{s[36:39]}"(s.k), "{v5}"(s.voff), "{s[40:43]}"(s.v)
#define SRC_K "v4, s[36:39], s86"
#define SRC_V "v5, s[40:43], s52"
#define LOADS(SRC) "buffer_load_dwordx4 v[%c[b0]:%c[b0]+3], " SRC " offen nt\n\tbuffer_load_dwordx4 v[%c[b0]+4:%c[b0]+7], " SRC " offen offset:1024 nt\n\t" \
                   "buffer_load_dwordx4 v[%c[b0]+8:%c[b0]+11], " SRC " offen offset:2048 nt\n\tbuffer_load_dwordx4 v[%c[b0]+12:%c[b0]+15], " SRC " offen offset:3072 nt"
#define GLDS4(SRC) "s_mov_b32 s87, m0\n\ts_mov_b32 m0, %[dst]\n\ts_nop 0\n\tbuffer_load_dwordx4 " SRC " offen nt lds\n\tbuffer_load_dwordx4 " SRC " offen offset:1024 nt lds\n\t" \
                   "buffer_load_dwordx4 " SRC " offen offset:2048 nt lds\n\tbuffer_load_dwordx4 " SRC " offen offset:3072 nt lds\n\ts_mov_b32 m0, s87"
#define ADV_K "s_add_u32 s86, s86, %c[adv]\n\t"
#define ADV_V "s_add_u32 s52, s52, %c[adv]\n\t"
template <bool V, int B, int ADV> DI void row_load(Att& s, unsigned dst = 0) {
#define RL(TXT) asm volatile(TXT : "+{s86}"(s.sok), "+{s52}"(s.sov) : KVIN, [b0] "i"(B < 0 ? 0 : B), [adv] "i"(ADV * 16384), [dst] "s"(dst) : "memory", "scc", "s87")
  if constexpr (B < 0) { if constexpr (V) RL(ADV_V GLDS4(SRC_V)); else RL(ADV_K GLDS4(SRC_K)); }
  else { if constexpr (V) RL(ADV_V LOADS(SRC_V)); else RL(ADV_K LOADS(SRC_K)); }
#undef RL
}
DI void buf_load(Att& s, v16u& buf) {
  asm volatile(ADV_K LOADS(SRC_K) : "+{s86}"(s.sok), "=&{v[8:23]}"(buf) : KVIN, [b0] "i"(8), [adv] "i"(16384) : "memory", "scc");
}
template <bool V, int B, int ADV, int W, bool SYNC> DI void row_step(Att& s, rsrc_t rs, unsigned dst = 0) {
#define US(PRE, REQ, POST) asm volatile("s_call_b64 s[94:95], " PRE "\n\t" REQ POST "\n\ts_waitcnt vmcnt(%c[w]+4)" \
    : "+{s86}"(s.sok), "+{s52}"(s.sov), "+{s88}"(s.sched), "+{s89}"(s.base), "+{s55}"(s.own), "+{s80}"(s.ld_off), "+{s85}"(s.prev), "+{s54}"(s.pmean), "+{s[56:71]}"(s.words) \
    : "{s90}"(s.start), "{s84}"(s.tau), "{s98}"(s.clk), "{s81}"(s.st_off), "{s74}"(s.tag), "{s[76:79]}"(rs), KVIN, [b0] "i"(B < 0 ? 0 : B), [adv] "i"(ADV * 16384), [w] "i"(W), [dst] "s"(dst) \
    : "memory", "scc", "vcc", "s87", "s91", "s92", "s93", "s94", "s95", "v50", "v51", "v52", ACL)
  if constexpr (B < 0) { if constexpr (V) US(".Lfd_pace", ADV_V GLDS4(SRC_V), ""); else US(".Lfd_pace", ADV_K GLDS4(SRC_K), ""); }
  else if constexpr (SYNC) { if constexpr (V) US(".Lfd_pace0", ADV_V LOADS(SRC_V), "\n\ts_call_b64 s[94:95], .Lfd_sync"); else US(".Lfd_pace0", ADV_K LOADS(SRC_K), "\n\ts_call_b64 s[94:95], .Lfd_sync"); }
  else { if constexpr (V) US(".Lfd_pace", ADV_V LOADS(SRC_V), ""); else US(".Lfd_pace", ADV_K LOADS(SRC_K), ""); }
#undef US
}
DI void hear(Att& s, rsrc_t rs) {
  asm volatile("s_call_b64 s[94:95], .Lfd_hear" : "+{s80}"(s.ld_off), "+{s[56:71]}"(s.words) : "{s98}"(s.clk), "{s[76:79]}"(rs) : "memory", "scc", "s92", "s93", "s94", "s95");
}
template <int A, int O> DI void buf_from_lds(Att& s, v16u& buf) {
  asm volatile("ds_read_b128 v[8:11], v[%c[a]] offset:%c[o]\n\tds_read_b128 v[12:15], v[%c[a]] offset:%c[o]+1024\n\tds_read_b128 v[16:19], v[%c[a]] offset:%c[o]+2048\n\t"
               "ds_read_b128 v[20:23], v[%c[a]] offset:%c[o]+3072" : "=&{v[8:23]}"(buf) : "{v1}"(s.lret), "{v2}"(s.lwin), [a] "i"(A), [o] "i"(O) : "memory");
}
template <int I> DI void exc_store(Att& s) { asm volatile("ds_write_b32 v3, v7 offset:%c2" ::"{v3}"(s.lexc), "{v7}"(s.e), "i"(I * 256) : "memory"); }
template <int I> DI void exc_load(Att& s) { asm volatile("ds_read_b32 v7, v3 offset:%c2\n\ts_waitcnt lgkmcnt(0)" : "={v7}"(s.e) : "{v3}"(s.lexc), "i"(I * 256) : "memory"); }
template <int I> DI void exc_load_scan(Att& s) { asm volatile("ds_read_b32 v53, v3 offset:%c2\n\ts_waitcnt lgkmcnt(0)" : "={v53}"(s.es) : "{v3}"(s.lexc), "i"(I * 256) : "memory"); }
#define PROC "v_mfma_f32_16x16x32_bf16 v[28:31], v[%c[bk]:%c[bk]+3], v[32:35], 0\n\t" \
             "v_mfma_f32_16x16x32_bf16 v[28:31], v[%c[bk]+4:%c[bk]+7], v[36:39], v[28:31]\n\t" \
             "s_call_b64 s[94:95], .Lfd_core\n\t" \
             "v_mfma_f32_16x16x32_bf16 v[44:47], v[%c[bv]:%c[bv]+3], v[54:57], v[44:47]\n\t" \
             "v_mfma_f32_16x16x32_bf16 v[44:47], v[%c[bv]+4:%c[bv]+7], v[58:61], v[44:47]\n\ts_nop 6\n\ts_waitcnt lgkmcnt(0)"
#define KPRC "v_mfma_f32_16x16x32_bf16 v[28:31], v[%c[bk]:%c[bk]+3], v[32:35], 0\n\tv_mfma_f32_16x16x32_bf16 v[54:57], v[%c[bk]+8:%c[bk]+11], v[32:35], 0\n\t" \
             "v_mfma_f32_16x16x32_bf16 v[28:31], v[%c[bk]+4:%c[bk]+7], v[36:39], v[28:31]\n\tv_mfma_f32_16x16x32_bf16 v[54:57], v[%c[bk]+12:%c[bk]+15], v[36:39], v[54:57]\n\t" \
             "s_call_b64 s[94:95], .Lfd_kred\n\tds_write_b32 v0, v50 offset:%c[o]\n\tds_write_b32 v0, v51 offset:%c[o]+64\n\tv_add_u32_e32 v0, %c[inc], v0\n\ts_waitcnt lgkmcnt(0)"
#define PROC_IO "+{v[44:47]}"(s.acc), "+{v48}"(s.m), "+{v49}"(s.l), "+{s98}"(s.clk)
#define PROC_IN "{v[32:35]}"(s.q0), "{v[36:39]}"(s.q1), "{v[40:43]}"(s.msk), "{s82}"(s.c2)
#define PROC_CL "memory", "vcc", "s94", "s95", "s99", "v28", "v29", "v30", "v31", "v50", "v51", "v52", "v54", "v55", "v56", "v57", "v58", "v59", "v60", "v61", ACL
#define T8_27 "v8", "v9", "v10", "v11", "v12", "v13", "v14", "v15", "v16", "v17", "v18", "v19", "v20", "v21", "v22", "v23", "v24", "v25", "v26", "v27"
template <int BK, int BV> DI void buf_proc(Att& s, v16u& buf) { asm volatile("s_waitcnt lgkmcnt(0)\n\t" PROC : PROC_IO : PROC_IN, "{v[8:23]}"(buf), [bk] "i"(BK), [bv] "i"(BV) : PROC_CL); }
template <int BK, int O, int INC> DI void k_proc(Att& s) { asm volatile(KPRC : "+{s98}"(s.clk), "+{v0}"(s.lsc) : PROC_IN, [bk] "i"(BK), [o] "i"(O), [inc] "i"(INC) : PROC_CL); }
template <int O, int INC> DI void k_proc_buf(Att& s, v16u& buf) {
  asm volatile("s_waitcnt lgkmcnt(0)\n\t" KPRC : "+{s98}"(s.clk), "+{v0}"(s.lsc) : PROC_IN, "{v[8:23]}"(buf), [bk] "i"(8), [o] "i"(O), [inc] "i"(INC) : PROC_CL);
}
DI void score_read(Att& s) { asm volatile("ds_read2_b32 v[62:63], v0 offset1:16\n\tv_add_u32_e32 v0, 128, v0" : "=&{v[62:63]}"(s.sc), "+{v0}"(s.lsc)::"memory"); }
template <int BV> DI void v_proc(Att& s) {
  asm volatile("s_waitcnt lgkmcnt(0)\n\ts_memrealtime s[98:99]\n\tv_mov_b32_e32 v50, v62\n\ts_call_b64 s[94:95], .Lfd_core3\n\t"
               "v_mfma_f32_16x16x32_bf16 v[44:47], v[%c[bv]:%c[bv]+3], v[54:57], v[44:47]\n\tv_mfma_f32_16x16x32_bf16 v[44:47], v[%c[bv]+4:%c[bv]+7], v[58:61], v[44:47]\n\t"
               "v_mov_b32_e32 v50, v63\n\ts_call_b64 s[94:95], .Lfd_core3\n\t"
               "v_mfma_f32_16x16x32_bf16 v[44:47], v[%c[bv]+8:%c[bv]+11], v[54:57], v[44:47]\n\tv_mfma_f32_16x16x32_bf16 v[44:47], v[%c[bv]+12:%c[bv]+15], v[58:61], v[44:47]\n\t"
               "s_nop 6\n\ts_waitcnt lgkmcnt(0)" : PROC_IO : PROC_IN, "{v[62:63]}"(s.sc), [bv] "i"(BV) : PROC_CL);
}
DI void pin_setup(Att& s) { asm volatile("" ::KVIN, "{v[40:43]}"(s.msk), "{v1}"(s.lret), "{v2}"(s.lwin), "{v3}"(s.lexc)); }
DI unsigned spread2(unsigned x) { x = (x | (x << 8)) & 0x00ff00ffu; x = (x | (x << 4)) & 0x0f0f0f0fu; return (x | (x << 2)) & 0x33333333u; }
#define REQK(B, ADV, W, S) row_step<false, B, ADV, W, S>(kv, rs)
#define REQV(B, ADV, W, S) row_step<true, B, ADV, W, S>(kv, rs)
#define REQLK(DST, W) row_step<false, -1, 1, W, false>(kv, rs, DST)
#define LOADK(B, ADV) row_load<false, B, ADV>(kv)
#define KPROC(B, O, INC) k_proc<B, O, INC>(kv)
#define VPROC(B) v_proc<B>(kv)
#define SREAD() score_read(kv)
#define SCORES(BASE) do { const unsigned w = opqs(w0); kv.lsc = BASE + (lane_id() >> 2) * 4; } while (0)
#define BUFK(A, O) buf_from_lds<A, O>(kv, buf)
#define PROCB(BK, BV) buf_proc<BK, BV>(kv, buf)
#define KPROCB(O, INC) k_proc_buf<O, INC>(kv, buf)
#define HEAR() hear(kv, rs)
#define PACK_(U, MV, C, B) asm volatile("s_call_b64 s[94:95], .Lfd_pack" #U "\n\t" MV : "+{v7}"(kv.e) \
    : "{s[44:51]}"(kv.kon), "{s72}"(C), "{s75}"(B), "{v1}"(kv.lret), "{v3}"(kv.lexc) : "memory", "vcc", "s94", "s95", "v50", "v51", "v52", "v62", "v63", T8_27, ACL)
#define PACKK(U, MV) PACK_(U, MV, kv.ck, kv.bk)
#define PACKV(U, MV) PACK_(U, MV, kv.cv, kv.bv)
#define FLAGSK(I) do { exc_store<I>(kv); kv.e = 0; } while (0)
#define FLAGSV(I, SH) do { exc_load_scan<I>(kv); kv.e = ((spread2(kv.es) << 2) | spread2(kv.e)) << SH; exc_store<I>(kv); kv.e = 0; } while (0)
#define UVPROC(POOL, SC, MV) asm volatile("s_waitcnt lgkmcnt(0)\n\tv_mov_b32_e32 v50, v" #SC "\n\tv_mov_b64_e32 v[8:9], v[62:63]\n\t" MV "\n\tv_add_u32_e32 v52, " #POOL ", v2\n\t" \
                                          "s_call_b64 s[94:95], .Lfd_uvproc\n\tv_mov_b64_e32 v[62:63], v[8:9]" \
    : "+{v[44:47]}"(kv.acc), "+{v48}"(kv.m), "+{v49}"(kv.l), "+{v7}"(kv.e), "+{v[62:63]}"(kv.sc) \
    : "{v[40:43]}"(kv.msk), "{s82}"(kv.c2), "{s[44:51]}"(kv.kon), "{s83}"(kv.bv), "{v2}"(kv.lwin) : PROC_CL, "s96", "s97", T8_27)
#define PPROC(POOL, MV) asm volatile(MV "\n\tv_add_u32_e32 v52, " #POOL ", v2\n\ts_call_b64 s[94:95], .Lfd_uproc" \
    : "+{v[44:47]}"(kv.acc), "+{v48}"(kv.m), "+{v49}"(kv.l), "+{s98}"(kv.clk), "+{v7}"(kv.e) \
    : "{v[32:35]}"(kv.q0), "{v[36:39]}"(kv.q1), "{v[40:43]}"(kv.msk), "{s82}"(kv.c2), "{s[44:51]}"(kv.kon), "{s75}"(kv.bk), "{s83}"(kv.bv), "{v1}"(kv.lret), "{v2}"(kv.lwin) \
    : PROC_CL, "s96", "s97", "v62", "v63", T8_27)
DI void packed_scan(Att& s, unsigned pool, unsigned unit_off) {
  asm volatile("v_cmp_lt_u32_e32 vcc, 0xfffffff, v53\n\ts_cbranch_vccz .Lfd_ns%=\n\ts_call_b64 s[94:95], .Lfd_scan\n.Lfd_ns%=:\n\tv_lshlrev_b32_e32 v53, 4, v53" : "+{v53}"(s.es) : KVIN, "{s96}"(pool), "{s97}"(unit_off) : "memory", "vcc", "scc", "s87", "s94", "s95");
}
template <int B> DI unsigned absmax8() {
  unsigned r, t;
  asm volatile("v_and_b32_e32 %0, 0x7fff7fff, v[%c2]\n\t"
               "v_and_b32_e32 %1, 0x7fff7fff, v[%c2+1]\n\tv_pk_max_u16 %0, %0, %1\n\tv_and_b32_e32 %1, 0x7fff7fff, v[%c2+2]\n\tv_pk_max_u16 %0, %0, %1\n\t"
               "v_and_b32_e32 %1, 0x7fff7fff, v[%c2+3]\n\tv_pk_max_u16 %0, %0, %1\n\tv_and_b32_e32 %1, 0x7fff7fff, v[%c2+4]\n\tv_pk_max_u16 %0, %0, %1\n\t"
               "v_and_b32_e32 %1, 0x7fff7fff, v[%c2+5]\n\tv_pk_max_u16 %0, %0, %1\n\tv_and_b32_e32 %1, 0x7fff7fff, v[%c2+6]\n\tv_pk_max_u16 %0, %0, %1\n\t"
               "v_and_b32_e32 %1, 0x7fff7fff, v[%c2+7]\n\tv_pk_max_u16 %0, %0, %1\n\ts_nop 1" : "=&v"(r), "=&v"(t) : "i"(B));
  r = max(r & 0xffffu, r >> 16);
  r = max(r, dppu<XOR1>(r)); r = max(r, dppu<XOR2>(r)); r = max(r, dppu<ROR4>(r)); r = max(r, dppu<ROR8>(r));
  auto s1 = __builtin_amdgcn_permlane16_swap(r, r, false, false); r = max(s1[0], s1[1]);
  auto s2 = __builtin_amdgcn_permlane32_swap(r, r, false, false); r = max(s2[0], s2[1]);
  return __builtin_amdgcn_readfirstlane(r);
}

DI void mfma1(f32x4& d, v4u a, v4u b) { asm volatile("s_nop 1\n\tv_mfma_f32_16x16x32_bf16 %0, %1, %2, %0" : "+v"(d) : "v"(a), "v"(b) : ACL); }
DI void mfma2(f32x4& d, v4u a0, v4u b0, v4u a1, v4u b1) {
  asm volatile("s_nop 1\n\tv_mfma_f32_16x16x32_bf16 %0, %1, %2, %0\n\tv_mfma_f32_16x16x32_bf16 %0, %3, %4, %0" : "+v"(d) : "v"(a0), "v"(b0), "v"(a1), "v"(b1) : ACL);
}
DI void mfma3(f32x4& d0, f32x4& d1, f32x4& d2, v4u a, v4u b0, v4u b1, v4u b2) {
  asm volatile("s_nop 1\n\tv_mfma_f32_16x16x32_bf16 %0, %3, %4, %0\n\tv_mfma_f32_16x16x32_bf16 %1, %3, %5, %1\n\tv_mfma_f32_16x16x32_bf16 %2, %3, %6, %2"
               : "+v"(d0), "+v"(d1), "+v"(d2) : "v"(a), "v"(b0), "v"(b1), "v"(b2) : ACL);
}
DI void mfma_fence(f32x4& d) { asm volatile("s_nop 7" : "+v"(d)::ACL); }


#define SB() __builtin_amdgcn_sched_barrier(0)
#define SETTLED() __builtin_amdgcn_s_waitcnt(0x0F70)
typedef const __attribute__((address_space(4))) Args* argp_t;
DI argp_t args_again() {
  argp_t p = (argp_t)__builtin_amdgcn_kernarg_segment_ptr();
  asm volatile("" : "+s"(p));
  return p;
}

__global__ void __launch_bounds__(256, 1) __attribute__((amdgpu_num_vgpr(CAP)))
fused_decode_kernel(Args a) {
  extern __shared__ __attribute__((aligned(16))) unsigned char lds_image[];
  asm volatile("" ::"v"((unsigned)(uintptr_t)(LAS unsigned char*)lds_image) : "v255", "a255");
  const unsigned w0 = __builtin_amdgcn_readfirstlane(threadIdx.x >> 6), wave0 = w0;
  const bool isP = ((blockIdx.x >> 3) & 3) == 0;
#define ATT_IDS kvh = 2 * (bi >> 2) + (xcd >> 2), sp = 4 * (xcd & 3) + (bi & 3)
#define LANES const int lane = lane_id(), g4 = lane >> 4, c16 = lane & 15; const int w = opqs(w0), bx = opqs(blockIdx.x), tid = w * 64 + lane; \
  const int xcd = bx & 7, bi = bx >> 3, ATT_IDS, pidx = 8 * xcd + (bi >> 2), fidx = 24 * xcd + (bi - (bi >> 2) - 1), ridx = (bi >> 2) * 8 + xcd; \
  (void)g4; (void)c16; (void)w; (void)tid; (void)pidx; (void)fidx; (void)ridx; (void)kvh; (void)sp

  const rsrc_t rs = mk_rsrc(a.ws, WS_END);
  Att kv;
  auto kv_lanes2 = [&](const unsigned char* kp, const unsigned char* vp) {
    LANES;
    const int nP_before = (sp + 3) >> 2;
    const size_t row0 = (size_t)kvh * (SK / 64) + nP_before * NPD + (sp - nP_before) * NFD;
    kv.k = mk_rsrc(kp + row0 * 16384, ((sp & 3) ? NFD : NPD) * 16384);
    kv.v = mk_rsrc(vp + row0 * 16384, ((sp & 3) ? NFD : NPD) * 16384);
    kv.koff = w * 4096 + (c16 >> 2) * 256 + g4 * 64 + (c16 & 3) * 16;
    kv.voff = w * 4096 + lane * 16;
  };
  auto kv_lanes = [&]() { const argp_t ap = args_again(); kv_lanes2(ap->k, ap->v); };
  kv_lanes2(a.k, a.v);
  kv.sok = 0u - 16384u; kv.sov = 0u - 16384u;
  auto first_units = [&]() { sfor<3>([&](auto I) { row_load<false, -1, 1>(kv, LDS_RET + (w0 * NL + I.value) * 4096); }); };
  if (!isP) first_units();
  const unsigned t_launch = (unsigned)__builtin_amdgcn_s_memrealtime() * 16u;

  auto load_wq = [&](const unsigned char* wq) {
    LANES;
    const rsrc_t r = mk_rsrc(wq + pidx * 128, DM * 8192 - pidx * 128);
    const unsigned o = (lane >> 3) * 8192 + (((lane & 7) ^ (((lane >> 4) & 3) << 1)) << 4);
#pragma unroll 1
    for (int pc = w; pc < 72; pc += 4) glds_b(o, r, pc * 65536, LDS_WIN + pc * 1024);
  };
  auto load_rows = [&](bool down) {
    LANES;
    const int npieces = down ? 27 : 72;
    const unsigned dst = down ? LDS_WIN + WIN_DN : LDS_WIN;
    const argp_t ap = args_again();
    const rsrc_t r = mk_rsrc(down ? ap->wd + (size_t)fidx * 24 * 1152 : ap->wo + (size_t)pidx * 64 * 1152, npieces * 1024);
#pragma unroll 1
    for (int pc = w; pc < npieces; pc += 4) {
      const unsigned idx = pc * 64 + lane, row = idx / 72u, cc = idx - row * 72u;
      glds_b(row * 1152 + ((cc ^ (((row >> 1) & 3) << 1)) << 4), r, 0, dst + pc * 1024);
    }
  };
  auto load_gu = [&](int first, int count) {
    LANES;
    const int l48 = lane < 48 ? lane : 0, r = l48 / 6, cc = l48 - r * 6;
    const argp_t ap = args_again();
    const uint64_t gb = (uint64_t)ap->wg, ub = (uint64_t)(ap->wu - 48);
    const unsigned off = cc * 16 + fidx * 48 + (8 * w + r) * (DF * 2);
    const unsigned lo = (cc < 3 ? (unsigned)gb : (unsigned)ub) + off, hi = (cc < 3 ? (unsigned)(gb >> 32) : (unsigned)(ub >> 32)) + (lo < off ? 1u : 0u);
    int i = first + ROT * (fidx % 3);
#pragma unroll 1
    for (int n = 0; n < count; ++n, ++i) {
      if (i >= 18) i -= 18;
      const unsigned d = i * (32 * DF * 2), l2 = lo + d, h2 = hi + (l2 < d ? 1u : 0u);
      glds_v48(v2u{l2, h2}, LDS_WIN + (w + 4 * i) * 768);
    }
  };
  auto refill_window = [&]() {
    const unsigned d = LDS_WIN + opqs(w0) * (NW * 4096);
    kv_lanes();
    kv.sok = opqs(0u - 16384u);
    row_load<false, -1, 1>(kv, d);
    kv.sok = opqs(((isP ? NPD : NFD) - 6) * 16384);
    row_load<false, -1, 1>(kv, d + 16384); row_load<false, -1, 2>(kv, d + 4096); row_load<false, -1, 1>(kv, d + 8192); row_load<false, -1, 1>(kv, d + 12288);
  };

  auto xh_addr = [&](int slot) { return LDS_XH + ((slot & ~7) + (slot & 3) * 2 + ((slot >> 2) & 1)) * 8; };
  v2u x0r;
  {
    LANES;
    if (isP) {
      const rsrc_t rx = mk_rsrc(a.x0, DM * 2);
      asm volatile("buffer_load_dwordx2 %0, %1, %2, 0 offen" : "=&v"(x0r) : "v"((36 * w + lane) * 8), "s"(rx) : "memory");
      load_wq(a.wq);
    }
    if (tid < 16) *LDSP(unsigned, LDS_ZERO + tid * 4) = 0u;
    if (tid < 144) *LDSP(v2u, LDS_ONES + tid * 8) = v2u{0x3F803F80u, 0x3F803F80u};
  }
  if (isP) {
    asm volatile("s_waitcnt vmcnt(18)" : "+v"(x0r)::"memory");
    LANES;
    if (lane < 36) *LDSP(v2u, xh_addr(36 * w + lane)) = x0r;
    vmwait<PWQ>();
    first_units();
  }
  define_subroutines();
  if ((blockIdx.x >> 3) == 3) prefetch_code(w0);

  const unsigned ep = *(const __attribute__((address_space(4))) unsigned*)(args_again()->ws + WS_EPOCH);
  const unsigned tagA = ep + 1u, tagB = (ep + 1u) * 0x9E3779B1u + 0x7F4A7C15u;
  bool dead = false;
  auto ok4 = [&](const v4u& v) { return v.y == tagA && v.w == tagB; };
  auto put = [&](unsigned off, unsigned v0, unsigned v1) { __builtin_amdgcn_raw_buffer_store_b128(v4u{v0, tagA, v1, tagB}, rs, off, 0, 16); };
  auto get = [&](unsigned off) { return __builtin_amdgcn_raw_buffer_load_b128(rs, off, 0, 16); };
  auto now16 = [&]() { return (unsigned)__builtin_amdgcn_s_memrealtime() * 16u; };
  auto pace_start = [&](int it, unsigned start) {
    const unsigned bx = opqs(blockIdx.x), w0 = opqs(wave0);
    const bool isP = ((bx >> 3) & 3) == 0;
    kv.start = start; kv.clk = start >> 4; kv.base = kv.start; kv.own = 0x7fffffff; kv.tau = it ? PACE2 : PACE; kv.sched = (isP && !it ? PSHIFT : 0) * PACE;
    kv.st_off = (w0 == 2 && ((bx >> 3) & 15) == 1) ? WS_SYNC + ((bx >> 7) * 8 + (bx & 7)) * 8 : ~0u;
    kv.ld_off = w0 == 0 ? WS_SYNC : ~0u;
    kv.tag = opqs(tagB) + it; kv.prev = 0; kv.pmean = 0;
    asm volatile("s_not_b32 s57, %1\n\ts_mov_b32 s59, s57\n\ts_mov_b32 s61, s57\n\ts_mov_b32 s63, s57\n\ts_mov_b32 s65, s57\n\ts_mov_b32 s67, s57\n\ts_mov_b32 s69, s57\n\ts_mov_b32 s71, s57"
                 : "=&{s[56:71]}"(kv.words) : "s"(kv.tag) : "scc");
  };

  auto gather_x = [&](unsigned base, int w, int lane) {
    const int slot = 36 * w + (lane < 36 ? lane : 0);
    v4u t;
    for (unsigned spin = 0;; ++spin) {
      asm volatile("" ::: "memory");
      t = get(base + (slot >> 2) * 128 + (slot & 3) * 16);
      if (__all(ok4(t)) || dead) break;
      if (spin > SPIN_CAP) { dead = true; break; }
    }
    SETTLED();
    return v2u{t.x, t.z};
  };
  auto stage_x = [&](v2u x, int w, int lane) {
    if (lane < 36) *LDSP(v2u, xh_addr(36 * w + lane)) = x;
    wg_barrier();
  };
  struct Stat { v4u a; v2u b; float s1, s2, mean, inv; };
  auto stat_step = [&](Stat& t, int step, int lane) {
    if (step == 0) { t.a = lds_r128(LDS_XH + lane * 16); t.b = *LDSP(v2u, lane < 16 ? LDS_XH + 1024 + lane * 8 : LDS_ZERO); }
    if (step == 1) {
      t.s1 = 0.f; t.s2 = 0.f;
#pragma unroll
      for (int i = 0; i < 4; ++i) { t.s1 = dot2(t.a[i], 0x3F803F80u, t.s1); t.s2 = dot2(t.a[i], t.a[i], t.s2); }
#pragma unroll
      for (int i = 0; i < 2; ++i) { t.s1 = dot2(t.b[i], 0x3F803F80u, t.s1); t.s2 = dot2(t.b[i], t.b[i], t.s2); }
    }
    if (step == 2) { t.s1 += dppf<XOR1>(t.s1); t.s2 += dppf<XOR1>(t.s2); t.s1 += dppf<XOR2>(t.s1); t.s2 += dppf<XOR2>(t.s2); t.s1 += dppf<ROR4>(t.s1); t.s2 += dppf<ROR4>(t.s2); t.s1 += dppf<ROR8>(t.s1); t.s2 += dppf<ROR8>(t.s2); }
    if (step == 3) { t.s1 = xrow_sum(t.s1); t.s2 = xrow_sum(t.s2); }
    if (step == 4) {
      const float md = t.s1 * (1.f / DM);
      t.mean = rbf(md);
      const float var = rbf(fmaxf(t.s2 * (1.f / DM) - md * md, 0.f));
      t.inv = __builtin_amdgcn_rcpf(rbf(__builtin_amdgcn_sqrtf(rbf(var + 1e-5f))));
      asm volatile("" : "+v"(t.mean), "+v"(t.inv));
    } else if (step > 0) asm volatile("" : "+v"(t.s1), "+v"(t.s2));
  };
  struct Proj { unsigned wa, second; };
  auto proj_setup = [&](auto KSc, int w, int lane) {
    constexpr int KS = KSc.value;
    const int g4 = lane >> 4, c16 = lane & 15;
    const unsigned hl = (2 * g4 + (c16 >> 3)) & 3, trow = 4 * g4 + (c16 >> 2);
    return Proj{(KS == 2 ? LDS_WIN : LDS_WIN + WIN_DN) + trow * 1152 + (((unsigned)w ^ hl) << 5) + (lane & 3) * 8,
                (KS == 1 && g4 >= 2) ? 8u * 1152 : 16u * 1152};
  };
  auto proj_frag = [&](auto KSc, const Proj& p, auto Nc) {
    constexpr int KS = KSc.value, n = Nc.value, tl = n / KS, ks = n % KS;
    constexpr int i = tl < 3 ? 4 * tl : (tl < 5 ? 1 + 4 * (tl - 3) : (tl < 7 ? 2 + 4 * (tl - 5) : 3 + 4 * (tl - 7)));
    return lds_frag(p.wa + i * 128 + ks * (32 * 1152), p.second);
  };
  auto proj_run = [&](auto KSc, auto PREc, const Proj& p, v4u* wf, unsigned ybase) {
    constexpr int KS = KSc.value, PRE = PREc.value, NFR = 9 * KS;
    LANES;
    const unsigned xsrc = KS == 2 ? LDS_XO : LDS_XO + w * 64, nprod = KS == 2 ? 64 : 192, prod = KS == 2 ? pidx : fidx;
    f32x4 d[3] = {{0, 0, 0, 0}, {0, 0, 0, 0}, {0, 0, 0, 0}};
    v4u av[2][KS];
    auto rdA = [&](int G) {
#pragma unroll
      for (int k = 0; k < KS; ++k) av[G & 1][k] = lds_r128((c16 >> 2) == G ? xsrc + 64 * k + g4 * 16 : LDS_ZERO);
    };
    rdA(0); rdA(1);
    SB();
    sfor<9>([&](auto Tc) {
      constexpr int tl = Tc.value, G = tl < 3 ? 0 : (tl < 5 ? 1 : (tl < 7 ? 2 : 3)), u = tl < 3 ? tl : ((tl - 3) & 1);
      if constexpr (KS == 2) mfma2(d[u], av[G & 1][0], wf[(2 * tl) % PRE], av[G & 1][1], wf[(2 * tl + 1) % PRE]);
      else mfma1(d[u], av[G & 1][0], wf[tl % PRE]);
      sfor<KS>([&](auto Kc) {
        constexpr int n = KS * tl + Kc.value + PRE;
        if constexpr (n < NFR) wf[n % PRE] = proj_frag(KSc, p, std::integral_constant<int, n>{});
      });
      if constexpr (tl == 2) rdA(2);
      if constexpr (tl == 4) rdA(3);
      SB();
    });
#pragma unroll
    for (int u = 0; u < 3; ++u) {
      mfma_fence(d[u]);
      const float nb = dppf<XOR1>(d[u][0]);
      const unsigned T = w + 4 * (4 * u + g4);
      if (!(lane & 1) && (u < 2 || g4 == 0)) put(ybase + (T * nprod + prod) * 128 + (c16 >> 1) * 16, __float_as_uint(d[u][0]), __float_as_uint(nb));
    }
  };
  auto reduce_cols = [&](auto NIc, auto WPTc, unsigned first, bool active, unsigned xold) {
    constexpr int NI = NIc.value, WPT = WPTc.value, STEP = WPT * 1024;
    LANES;
    if (active) {
      v4u t[NI];
#pragma unroll
      for (int i = 0; i < NI; ++i) t[i] = get(first + i * STEP);
      for (unsigned spin = 0;; ++spin) {
        bool ok = true;
#pragma unroll
        for (int i = 0; i < NI; ++i) ok = ok && ok4(t[i]);
        if (__all(ok) || dead) break;
        if (spin > SPIN_CAP) { dead = true; break; }
        asm volatile("" ::: "memory");
#pragma unroll
        for (int i = 0; i < NI; ++i) if (!ok4(t[i])) t[i] = get(first + i * STEP);
      }
      SETTLED();
      float s0 = 0.f, s1 = 0.f;
#pragma unroll
      for (int i = 0; i < NI; ++i) { s0 += __uint_as_float(t[i].x); s1 += __uint_as_float(t[i].z); }
      s0 += dppf<ROR8>(s0); s1 += dppf<ROR8>(s1);
      s0 = xrow_sum(s0); s1 = xrow_sum(s1);
      if (lane < 16) *LDSP(float, LDS_RED + ((lane & 7) * 2 + (lane >> 3)) * 16 + w * 4) = lane < 8 ? s0 : s1;
    }
    wg_barrier();
    const f32x4 r = *LDSP(f32x4, LDS_RED + c16 * 16);
    const float y = WPT == 4 ? (r[0] + r[1]) + (r[2] + r[3]) : (w < 2 ? r[0] + r[1] : r[2] + r[3]);
    const unsigned xres = cvtpk(bflo(xold) + rbf(y), 0.f) & 0xffffu;
    return v2u{xres | (dppu<QP1>(xres) << 16), dppu<QP2>(xres) | (dppu<QP3>(xres) << 16)};
  };
  constexpr std::integral_constant<int, 1> I1{};
  constexpr std::integral_constant<int, 2> I2{};

#pragma unroll 1
  for (int it = 0; it < 2; ++it) {
    if (isP) {
      constexpr int PRE = 9;
      LANES;
      if (it == 0) vmwait<12>(); else vmwait<0>();
      wg_barrier();
      const unsigned hl = (2 * g4 + (c16 >> 3)) & 3, trow = 4 * g4 + (c16 >> 2);
      const unsigned wa = LDS_WIN + trow * 128 + (((unsigned)w ^ hl) << 5) + (lane & 3) * 8;
      v4u wf[PRE];
#pragma unroll
      for (int i = 0; i < PRE; ++i) wf[i] = lds_frag(wa + i * 4096, 2048);
      const v2u xr = it == 1 ? gather_x(WS_X + 1 * X_BYTES, w, lane) : *LDSP(v2u, xh_addr(36 * w + (lane < 36 ? lane : 0)));
      stage_x(xr, w, lane);
      const unsigned xa = (c16 < 8 ? LDS_XH : LDS_ONES) + g4 * 16;
      f32x4 d0 = {0, 0, 0, 0}, d1 = {0, 0, 0, 0};
      v4u av[3];
#pragma unroll
      for (int i = 0; i < 3; ++i) av[i] = lds_r128(xa + i * 64);
      Stat st;
      SB();
      sfor<18>([&](auto Kc) {
        constexpr int ks = Kc.value;
        mfma1((ks & 1) ? d1 : d0, av[ks % 3], wf[ks % PRE]);
        if constexpr (ks + 3 < 18) av[ks % 3] = lds_r128(xa + (ks + 3) * 64);
        if constexpr (ks + PRE < 18) wf[ks % PRE] = lds_frag(wa + (ks + PRE) * 4096, 2048);
        if constexpr (ks >= 9 && (ks & 1) && ks < 18) stat_step(st, (ks - 9) / 2, lane);
        SB();
      });
      const float mean = st.mean, inv = st.inv;
      mfma_fence(d0); mfma_fence(d1);
      const unsigned sv = __float_as_uint(d0[0] + d1[0]);
      auto sw = __builtin_amdgcn_permlane32_swap(sv, sv, false, false);
      const unsigned qb = cvtpk((__uint_as_float(sw[0]) - mean * __uint_as_float(sw[1])) * inv, 0.f) & 0xffffu;
      const unsigned v0 = qb | (dppu<QP1>(qb) << 16), v1 = dppu<QP2>(qb) | (dppu<QP3>(qb) << 16);
      if (lane < 16 && !(lane & 3)) put(WS_Q + it * Q_BYTES + (pidx * 4 + w) * 128 + (lane >> 2) * 16, v0, v1);
      wg_barrier();
      if (it == 1) refill_window();
    }

    unsigned qoff;
    auto att_setup = [&]() {
      kv_lanes();
      LANES;
#pragma unroll
      for (int i = 0; i < 4; ++i) kv.msk[i] = ((c16 & 7) >> 1) == i ? ((c16 & 1) ? 0xffff0000u : 0xffffu) : 0u;
      qoff = WS_Q + it * Q_BYTES + ((2 * kvh + (c16 >> 3)) * 8 + 2 * g4 + ((c16 >> 1) & 1)) * 128 + (c16 & 1) * 32;
      kv.lret = LDS_RET + w * (NL * 4096) + lane * 16; kv.lwin = LDS_WIN + w * (NW * 4096) + lane * 16; kv.lexc = LDS_EXC + w * 1280 + lane * 4;
      kv.c2 = C2;
      v8u& kon = kv.kon;
      asm volatile("s_mov_b32 s44, 0x06040200\n\ts_mov_b32 s45, 0x07050301\n\ts_mov_b32 s46, 0x05010400\n\ts_mov_b32 s47, 0x07030602\n\t"
                   "s_mov_b32 s48, 0x80808080\n\ts_mov_b32 s49, %1\n\ts_mov_b32 s50, 0x0f0f0f0f\n\ts_mov_b32 s51, 0xf8f8f8f8" : "=&{s[44:51]}"(kon) : "s"(it ? 0x7f7f7f7fu : 0x07070707u));
    };
    auto q_issue = [&](v4u& t0, v4u& t1) {
      asm volatile("buffer_load_dwordx4 %0, %2, %3, 0 offen sc1\n\tbuffer_load_dwordx4 %1, %2, %3, 0 offen offset:16 sc1" : "=&v"(t0), "=&v"(t1) : "v"(qoff), "s"(rs) : "memory");
    };
    auto q_finish = [&](v4u& t0, v4u& t1) {
      for (unsigned spin = 0; !(__all(ok4(t0) && ok4(t1)) || dead); ++spin) {
        if (spin > SPIN_CAP) { dead = true; break; }
        q_issue(t0, t1);
        asm volatile("s_waitcnt vmcnt(0)" : "+v"(t0), "+v"(t1)::"memory");
      }
      const v4u q = {t0.x, t0.z, t1.x, t1.z}, z = {0, 0, 0, 0};
      const bool hi = (lane_id() & 4) != 0;
      kv.q0 = hi ? z : q; kv.q1 = hi ? q : z;
      kv.acc = f32x4{0, 0, 0, 0}; kv.m = -3.0e38f; kv.l = 0.f;
      asm volatile("s_nop 1" : "+{v[44:47]}"(kv.acc), "+{v48}"(kv.m), "+{v49}"(kv.l), "+{v[32:35]}"(kv.q0), "+{v[36:39]}"(kv.q1), "+{v[40:43]}"(kv.msk));
    };
    auto window = [&](unsigned amax) { return opqs(__builtin_amdgcn_readfirstlane(max((int)min((amax + 0x40) >> 8, 127u) - 7, 1) * 0x01010101u)); };
    auto v_window = [&](auto Bc, unsigned wl) {
      kv.bv = window(absmax8<Bc.value>()); kv.cv = opqs(0x80808080u - kv.bv);
      *LDSP(unsigned, LDS_BASE + wl * 8 + 4) = kv.bv;
    };
    auto first_q = [&](v4u& t0, v4u& t1, unsigned wl) {
      asm volatile("s_waitcnt vmcnt(12)" : "+v"(t0), "+v"(t1)::"memory");
      kv.bk = window(absmax8<208>()); kv.ck = opqs(0x80808080u - kv.bk); kv.e = 0;
      *LDSP(unsigned, LDS_BASE + wl * 8) = kv.bk;
      q_finish(t0, t1);
    };
    if (it == 0) {
      att_setup();
      pace_start(0, t_launch);
      const unsigned wl = opqs(w0);
      v4u t0, t1;
      REQLK(LDS_RET + (wl * 4 + 3) * 4096, 52);
      REQLK(LDS_WIN + (wl * 5 + 0) * 4096, 52);
      REQLK(LDS_WIN + (wl * 5 + 1) * 4096, 52);
      REQLK(LDS_WIN + (wl * 5 + 2) * 4096, 52);
      REQLK(LDS_WIN + (wl * 5 + 3) * 4096, 52);
      REQLK(LDS_WIN + (wl * 5 + 4) * 4096, 52);
      REQK(208, 1, 52, 0);
      q_issue(t0, t1);
      REQK(224, 1, 52, 0);
      REQK(240, 1, 52, 0);
      REQK(64, 1, 52, 0);
      REQK(80, 1, 52, 0);
      first_q(t0, t1, wl);
      SCORES(LDS_WIN + w * (NW * 4096) + 12288);
      { v16u buf; BUFK(2, 12288); REQK(96, 1, 8, 0); KPROCB(896, 0); }
      { v16u buf; BUFK(2, 16384); REQK(112, 1, 8, 0); KPROCB(1024, 0); KPROC(64, 1536, 0); }
      REQK(64, 1, 8, 1); KPROC(208, 1152, 0); KPROC(80, 1664, 0);
      REQK(80, 1, 8, 0); KPROC(224, 1280, 0); KPROC(96, 1792, 0); PACKK(2, "v_accvgpr_write_b32 a0, v8\n\tv_accvgpr_write_b32 a1, v9\n\tv_accvgpr_write_b32 a2, v10\n\tv_accvgpr_write_b32 a3, v11\n\tv_accvgpr_write_b32 a4, v12\n\tv_accvgpr_write_b32 a5, v13\n\tv_accvgpr_write_b32 a6, v14\n\tv_accvgpr_write_b32 a7, v15\n\tv_accvgpr_write_b32 a8, v16\n\tv_accvgpr_write_b32 a9, v17\n\tv_accvgpr_write_b32 a10, v18\n\tv_accvgpr_write_b32 a11, v19"); HEAR();
      REQK(96, 1, 8, 0); KPROC(240, 1408, 0); KPROC(112, 1920, 0);
      { v16u buf; BUFK(2, 0); REQK(112, 1, 8, 0); KPROCB(512, 0); KPROC(64, 2048, 0); PACKK(0, "v_accvgpr_write_b32 a12, v8\n\tv_accvgpr_write_b32 a13, v9\n\tv_accvgpr_write_b32 a14, v10\n\tv_accvgpr_write_b32 a15, v11\n\tv_accvgpr_write_b32 a16, v12\n\tv_accvgpr_write_b32 a17, v13\n\tv_accvgpr_write_b32 a18, v14\n\tv_accvgpr_write_b32 a19, v15\n\tv_accvgpr_write_b32 a20, v16\n\tv_accvgpr_write_b32 a21, v17\n\tv_accvgpr_write_b32 a22, v18\n\tv_accvgpr_write_b32 a23, v19"); }
      { v16u buf; BUFK(2, 4096); REQK(64, 1, 8, 1); KPROCB(640, 0); KPROC(80, 2176, 0); }
      { v16u buf; BUFK(2, 8192); REQK(80, 1, 8, 0); KPROCB(768, 0); KPROC(96, 2304, 0); PACKK(2, "v_accvgpr_write_b32 a24, v8\n\tv_accvgpr_write_b32 a25, v9\n\tv_accvgpr_write_b32 a26, v10\n\tv_accvgpr_write_b32 a27, v11\n\tv_accvgpr_write_b32 a28, v12\n\tv_accvgpr_write_b32 a29, v13\n\tv_accvgpr_write_b32 a30, v14\n\tv_accvgpr_write_b32 a31, v15\n\tv_accvgpr_write_b32 a32, v16\n\tv_accvgpr_write_b32 a33, v17\n\tv_accvgpr_write_b32 a34, v18\n\tv_accvgpr_write_b32 a35, v19"); HEAR(); }
      { v16u buf; BUFK(1, 0); REQK(96, 1, 8, 0); KPROCB(0, 0); KPROC(112, 2432, 0); }
      { v16u buf; BUFK(1, 4096); REQK(112, 1, 8, 0); KPROCB(128, 0); KPROC(64, 2560, 0); PACKK(0, "v_accvgpr_write_b32 a36, v8\n\tv_accvgpr_write_b32 a37, v9\n\tv_accvgpr_write_b32 a38, v10\n\tv_accvgpr_write_b32 a39, v11\n\tv_accvgpr_write_b32 a40, v12\n\tv_accvgpr_write_b32 a41, v13\n\tv_accvgpr_write_b32 a42, v14\n\tv_accvgpr_write_b32 a43, v15\n\tv_accvgpr_write_b32 a44, v16\n\tv_accvgpr_write_b32 a45, v17\n\tv_accvgpr_write_b32 a46, v18\n\tv_accvgpr_write_b32 a47, v19"); FLAGSK(0); }
      { v16u buf; BUFK(1, 8192); REQK(64, 1, 8, 1); KPROCB(256, 0); KPROC(80, 2688, 0); }
      { v16u buf; BUFK(1, 12288); REQK(80, 1, 8, 0); KPROCB(384, 0); KPROC(96, 2816, 0); PACKK(2, "v_accvgpr_write_b32 a48, v8\n\tv_accvgpr_write_b32 a49, v9\n\tv_accvgpr_write_b32 a50, v10\n\tv_accvgpr_write_b32 a51, v11\n\tv_accvgpr_write_b32 a52, v12\n\tv_accvgpr_write_b32 a53, v13\n\tv_accvgpr_write_b32 a54, v14\n\tv_accvgpr_write_b32 a55, v15\n\tv_accvgpr_write_b32 a56, v16\n\tv_accvgpr_write_b32 a57, v17\n\tv_accvgpr_write_b32 a58, v18\n\tv_accvgpr_write_b32 a59, v19"); HEAR(); }
      REQK(96, 1, 8, 0); KPROC(112, 2944, 0);
      REQK(112, 1, 8, 0); SCORES(LDS_WIN + w * (NW * 4096) + 12288 + 24 * 128); KPROC(64, 0, 128); PACKK(0, "v_accvgpr_write_b32 a60, v8\n\tv_accvgpr_write_b32 a61, v9\n\tv_accvgpr_write_b32 a62, v10\n\tv_accvgpr_write_b32 a63, v11\n\tv_accvgpr_write_b32 a64, v12\n\tv_accvgpr_write_b32 a65, v13\n\tv_accvgpr_write_b32 a66, v14\n\tv_accvgpr_write_b32 a67, v15\n\tv_accvgpr_write_b32 a68, v16\n\tv_accvgpr_write_b32 a69, v17\n\tv_accvgpr_write_b32 a70, v18\n\tv_accvgpr_write_b32 a71, v19");
      REQK(64, 1, 8, 1); KPROC(80, 0, 128);
      REQK(80, 1, 8, 0); KPROC(96, 0, 128); PACKK(2, "v_accvgpr_write_b32 a72, v8\n\tv_accvgpr_write_b32 a73, v9\n\tv_accvgpr_write_b32 a74, v10\n\tv_accvgpr_write_b32 a75, v11\n\tv_accvgpr_write_b32 a76, v12\n\tv_accvgpr_write_b32 a77, v13\n\tv_accvgpr_write_b32 a78, v14\n\tv_accvgpr_write_b32 a79, v15\n\tv_accvgpr_write_b32 a80, v16\n\tv_accvgpr_write_b32 a81, v17\n\tv_accvgpr_write_b32 a82, v18\n\tv_accvgpr_write_b32 a83, v19"); HEAR();
      REQK(96, 1, 8, 0); KPROC(112, 0, 128);
      REQK(112, 1, 8, 0); KPROC(64, 0, 128); PACKK(0, "v_accvgpr_write_b32 a84, v8\n\tv_accvgpr_write_b32 a85, v9\n\tv_accvgpr_write_b32 a86, v10\n\tv_accvgpr_write_b32 a87, v11\n\tv_accvgpr_write_b32 a88, v12\n\tv_accvgpr_write_b32 a89, v13\n\tv_accvgpr_write_b32 a90, v14\n\tv_accvgpr_write_b32 a91, v15\n\tv_accvgpr_write_b32 a92, v16\n\tv_accvgpr_write_b32 a93, v17\n\tv_accvgpr_write_b32 a94, v18\n\tv_accvgpr_write_b32 a95, v19"); FLAGSK(1);
      REQK(64, 1, 8, 1); KPROC(80, 0, 128);
      REQK(80, 1, 8, 0); KPROC(96, 0, 128); PACKK(2, "v_accvgpr_write_b32 a96, v8\n\tv_accvgpr_write_b32 a97, v9\n\tv_accvgpr_write_b32 a98, v10\n\tv_accvgpr_write_b32 a99, v11\n\tv_accvgpr_write_b32 a100, v12\n\tv_accvgpr_write_b32 a101, v13\n\tv_accvgpr_write_b32 a102, v14\n\tv_accvgpr_write_b32 a103, v15\n\tv_accvgpr_write_b32 a104, v16\n\tv_accvgpr_write_b32 a105, v17\n\tv_accvgpr_write_b32 a106, v18\n\tv_accvgpr_write_b32 a107, v19"); HEAR();
      REQK(96, 1, 8, 0); KPROC(112, 0, 128);
      REQK(112, 1, 8, 0); KPROC(64, 0, 128); PACKK(0, "v_accvgpr_write_b32 a108, v8\n\tv_accvgpr_write_b32 a109, v9\n\tv_accvgpr_write_b32 a110, v10\n\tv_accvgpr_write_b32 a111, v11\n\tv_accvgpr_write_b32 a112, v12\n\tv_accvgpr_write_b32 a113, v13\n\tv_accvgpr_write_b32 a114, v14\n\tv_accvgpr_write_b32 a115, v15\n\tv_accvgpr_write_b32 a116, v16\n\tv_accvgpr_write_b32 a117, v17\n\tv_accvgpr_write_b32 a118, v18\n\tv_accvgpr_write_b32 a119, v19");
      REQK(64, 1, 8, 1); KPROC(80, 0, 128);
      REQK(80, 1, 8, 0); KPROC(96, 0, 128); PACKK(2, "v_accvgpr_write_b32 a120, v8\n\tv_accvgpr_write_b32 a121, v9\n\tv_accvgpr_write_b32 a122, v10\n\tv_accvgpr_write_b32 a123, v11\n\tv_accvgpr_write_b32 a124, v12\n\tv_accvgpr_write_b32 a125, v13\n\tv_accvgpr_write_b32 a126, v14\n\tv_accvgpr_write_b32 a127, v15\n\tv_accvgpr_write_b32 a128, v16\n\tv_accvgpr_write_b32 a129, v17\n\tv_accvgpr_write_b32 a130, v18\n\tv_accvgpr_write_b32 a131, v19"); HEAR();
      REQK(96, 1, 8, 0); KPROC(112, 0, 128);
      REQK(112, 1, 8, 0); KPROC(64, 0, 128); PACKK(0, "v_accvgpr_write_b32 a132, v8\n\tv_accvgpr_write_b32 a133, v9\n\tv_accvgpr_write_b32 a134, v10\n\tv_accvgpr_write_b32 a135, v11\n\tv_accvgpr_write_b32 a136, v12\n\tv_accvgpr_write_b32 a137, v13\n\tv_accvgpr_write_b32 a138, v14\n\tv_accvgpr_write_b32 a139, v15\n\tv_accvgpr_write_b32 a140, v16\n\tv_accvgpr_write_b32 a141, v17\n\tv_accvgpr_write_b32 a142, v18\n\tv_accvgpr_write_b32 a143, v19"); FLAGSK(2);
      REQK(64, 1, 8, 1); KPROC(80, 0, 128);
      REQK(80, 1, 8, 0); KPROC(96, 0, 128); PACKK(2, "v_accvgpr_write_b32 a144, v8\n\tv_accvgpr_write_b32 a145, v9\n\tv_accvgpr_write_b32 a146, v10\n\tv_accvgpr_write_b32 a147, v11\n\tv_accvgpr_write_b32 a148, v12\n\tv_accvgpr_write_b32 a149, v13\n\tv_accvgpr_write_b32 a150, v14\n\tv_accvgpr_write_b32 a151, v15\n\tv_accvgpr_write_b32 a152, v16\n\tv_accvgpr_write_b32 a153, v17\n\tv_accvgpr_write_b32 a154, v18\n\tv_accvgpr_write_b32 a155, v19"); HEAR();
      REQK(96, 1, 8, 0); KPROC(112, 0, 128);
      REQK(112, 1, 8, 0); KPROC(64, 0, 128); PACKK(0, "ds_write_b128 v1, v[8:11] offset:0\n\tds_write_b128 v1, v[12:15] offset:1024\n\tds_write_b128 v1, v[16:19] offset:2048\n\ts_waitcnt lgkmcnt(0)");
      REQK(64, 1, 8, 1); KPROC(80, 0, 128);
      REQK(80, 1, 8, 0); KPROC(96, 0, 128); PACKK(2, "ds_write_b128 v1, v[8:11] offset:3072\n\tds_write_b128 v1, v[12:15] offset:4096\n\tds_write_b128 v1, v[16:19] offset:5120\n\ts_waitcnt lgkmcnt(0)"); HEAR();
      REQK(96, 1, 8, 0); KPROC(112, 0, 128);
      REQK(112, 1, 8, 0); KPROC(64, 0, 128); PACKK(0, "ds_write_b128 v1, v[8:11] offset:6144\n\tds_write_b128 v1, v[12:15] offset:7168\n\tds_write_b128 v1, v[16:19] offset:8192\n\ts_waitcnt lgkmcnt(0)"); FLAGSK(3);
      REQK(64, 1, 8, 1); KPROC(80, 0, 128);
      REQK(80, 1, 8, 0); KPROC(96, 0, 128); PACKK(2, "ds_write_b128 v1, v[8:11] offset:9216\n\tds_write_b128 v1, v[12:15] offset:10240\n\tds_write_b128 v1, v[16:19] offset:11264\n\ts_waitcnt lgkmcnt(0)"); HEAR();
      REQK(96, 1, 8, 0); KPROC(112, 0, 128);
      REQK(112, 1, 8, 0); KPROC(64, 0, 128); PACKK(0, "ds_write_b128 v1, v[8:11] offset:12288\n\tds_write_b128 v1, v[12:15] offset:13312\n\tds_write_b128 v1, v[16:19] offset:14336\n\ts_waitcnt lgkmcnt(0)"); kv.e <<= 4; FLAGSK(4);
      REQK(64, 1, 8, 1); KPROC(80, 0, 128);
      REQK(80, 1, 8, 0); KPROC(96, 0, 128); HEAR();
      if (!isP) {
        REQK(96, 1, 8, 0); KPROC(112, 0, 128);
        REQK(112, 1, 8, 0); KPROC(64, 0, 128);
        REQK(64, 1, 8, 1); KPROC(80, 0, 128);
        REQK(80, 1, 8, 0); KPROC(96, 0, 128); HEAR();
      }
      REQK(96, 1, 8, 0); KPROC(112, 0, 128);
      REQK(112, 1, 8, 0); KPROC(64, 0, 128);
      REQK(64, 1, 8, 1); SCORES(LDS_CMB + w * 1024); KPROC(80, 0, 128);
      REQK(80, 1, 8, 0); KPROC(96, 0, 128); HEAR();
      REQK(96, 1, 8, 0); KPROC(112, 0, 128);
      REQK(112, 1, 8, 0); KPROC(64, 0, 128);
      REQK(64, 1, 8, 1); KPROC(80, 0, 128);
      REQV(80, 1, 8, 0); KPROC(96, 0, 128); HEAR();
      REQV(96, 1, 8, 0); KPROC(112, 0, 128);
      REQV(112, 1, 8, 0); KPROC(64, 0, 128);
      SCORES(LDS_WIN + w * (NW * 4096) + 12288); SREAD(); REQV(64, 1, 8, 1); VPROC(80); v_window(IC<80>{}, wl);
      SREAD(); REQV(80, 1, 8, 0); VPROC(96); HEAR();
      SREAD(); REQV(96, 1, 8, 0); VPROC(112);
      SREAD(); REQV(112, 1, 8, 0); VPROC(64);
      SREAD(); REQV(64, 1, 8, 1); VPROC(80);
      SREAD(); REQV(80, 1, 8, 0); VPROC(96); HEAR();
      SREAD(); REQV(96, 1, 8, 0); VPROC(112);
      SREAD(); REQV(112, 1, 8, 0); VPROC(64);
      SREAD(); REQV(64, 1, 8, 1); VPROC(80);
      SREAD(); REQV(80, 1, 8, 0); VPROC(96); HEAR();
      SREAD(); REQV(96, 1, 8, 0); VPROC(112);
      SREAD(); REQV(112, 1, 8, 0); VPROC(64);
      SREAD(); REQV(64, 1, 8, 1); VPROC(80);
      SREAD(); REQV(80, 1, 8, 0); VPROC(96); HEAR();
      SREAD(); REQV(96, 1, 8, 0); VPROC(112); PACKV(3, "v_accvgpr_write_b32 a156, v8\n\tv_accvgpr_write_b32 a157, v9\n\tv_accvgpr_write_b32 a158, v10\n\tv_accvgpr_write_b32 a159, v11\n\tv_accvgpr_write_b32 a160, v12\n\tv_accvgpr_write_b32 a161, v13\n\tv_accvgpr_write_b32 a162, v14\n\tv_accvgpr_write_b32 a163, v15\n\tv_accvgpr_write_b32 a164, v16\n\tv_accvgpr_write_b32 a165, v17\n\tv_accvgpr_write_b32 a166, v18\n\tv_accvgpr_write_b32 a167, v19");
      SREAD(); REQV(112, 1, 8, 0); VPROC(64);
      SREAD(); REQV(64, 1, 8, 1); VPROC(80); PACKV(1, "v_accvgpr_write_b32 a168, v8\n\tv_accvgpr_write_b32 a169, v9\n\tv_accvgpr_write_b32 a170, v10\n\tv_accvgpr_write_b32 a171, v11\n\tv_accvgpr_write_b32 a172, v12\n\tv_accvgpr_write_b32 a173, v13\n\tv_accvgpr_write_b32 a174, v14\n\tv_accvgpr_write_b32 a175, v15\n\tv_accvgpr_write_b32 a176, v16\n\tv_accvgpr_write_b32 a177, v17\n\tv_accvgpr_write_b32 a178, v18\n\tv_accvgpr_write_b32 a179, v19");
      SREAD(); REQV(80, 1, 8, 0); VPROC(96); HEAR();
      SREAD(); REQV(96, 1, 8, 0); VPROC(112); PACKV(3, "v_accvgpr_write_b32 a180, v8\n\tv_accvgpr_write_b32 a181, v9\n\tv_accvgpr_write_b32 a182, v10\n\tv_accvgpr_write_b32 a183, v11\n\tv_accvgpr_write_b32 a184, v12\n\tv_accvgpr_write_b32 a185, v13\n\tv_accvgpr_write_b32 a186, v14\n\tv_accvgpr_write_b32 a187, v15\n\tv_accvgpr_write_b32 a188, v16\n\tv_accvgpr_write_b32 a189, v17\n\tv_accvgpr_write_b32 a190, v18\n\tv_accvgpr_write_b32 a191, v19");
      SREAD(); REQV(112, 1, 8, 0); VPROC(64);
      SREAD(); REQV(64, 1, 8, 1); VPROC(80); PACKV(1, "v_accvgpr_write_b32 a192, v8\n\tv_accvgpr_write_b32 a193, v9\n\tv_accvgpr_write_b32 a194, v10\n\tv_accvgpr_write_b32 a195, v11\n\tv_accvgpr_write_b32 a196, v12\n\tv_accvgpr_write_b32 a197, v13\n\tv_accvgpr_write_b32 a198, v14\n\tv_accvgpr_write_b32 a199, v15\n\tv_accvgpr_write_b32 a200, v16\n\tv_accvgpr_write_b32 a201, v17\n\tv_accvgpr_write_b32 a202, v18\n\tv_accvgpr_write_b32 a203, v19"); FLAGSV(0, 0);
      SREAD(); REQV(80, 1, 8, 0); VPROC(96); HEAR();
      SREAD(); REQV(96, 1, 8, 0); VPROC(112); PACKV(3, "v_accvgpr_write_b32 a204, v8\n\tv_accvgpr_write_b32 a205, v9\n\tv_accvgpr_write_b32 a206, v10\n\tv_accvgpr_write_b32 a207, v11\n\tv_accvgpr_write_b32 a208, v12\n\tv_accvgpr_write_b32 a209, v13\n\tv_accvgpr_write_b32 a210, v14\n\tv_accvgpr_write_b32 a211, v15\n\tv_accvgpr_write_b32 a212, v16\n\tv_accvgpr_write_b32 a213, v17\n\tv_accvgpr_write_b32 a214, v18\n\tv_accvgpr_write_b32 a215, v19");
      SREAD(); REQV(112, 1, 8, 0); VPROC(64);
      SREAD(); REQV(64, 1, 8, 1); VPROC(80); PACKV(1, "v_accvgpr_write_b32 a216, v8\n\tv_accvgpr_write_b32 a217, v9\n\tv_accvgpr_write_b32 a218, v10\n\tv_accvgpr_write_b32 a219, v11\n\tv_accvgpr_write_b32 a220, v12\n\tv_accvgpr_write_b32 a221, v13\n\tv_accvgpr_write_b32 a222, v14\n\tv_accvgpr_write_b32 a223, v15\n\tv_accvgpr_write_b32 a224, v16\n\tv_accvgpr_write_b32 a225, v17\n\tv_accvgpr_write_b32 a226, v18\n\tv_accvgpr_write_b32 a227, v19");
      SREAD(); REQV(80, 1, 8, 0); VPROC(96); HEAR();
      SREAD(); REQV(96, 1, 8, 0); VPROC(112); PACKV(3, "v_accvgpr_write_b32 a228, v8\n\tv_accvgpr_write_b32 a229, v9\n\tv_accvgpr_write_b32 a230, v10\n\tv_accvgpr_write_b32 a231, v11\n\tv_accvgpr_write_b32 a232, v12\n\tv_accvgpr_write_b32 a233, v13\n\tv_accvgpr_write_b32 a234, v14\n\tv_accvgpr_write_b32 a235, v15\n\tv_accvgpr_write_b32 a236, v16\n\tv_accvgpr_write_b32 a237, v17\n\tv_accvgpr_write_b32 a238, v18\n\tv_accvgpr_write_b32 a239, v19");
      SREAD(); REQV(112, 1, 8, 0); VPROC(64);
      SREAD(); REQV(64, 1, 8, 1); VPROC(80); PACKV(1, "v_accvgpr_write_b32 a240, v8\n\tv_accvgpr_write_b32 a241, v9\n\tv_accvgpr_write_b32 a242, v10\n\tv_accvgpr_write_b32 a243, v11\n\tv_accvgpr_write_b32 a244, v12\n\tv_accvgpr_write_b32 a245, v13\n\tv_accvgpr_write_b32 a246, v14\n\tv_accvgpr_write_b32 a247, v15\n\tv_accvgpr_write_b32 a248, v16\n\tv_accvgpr_write_b32 a249, v17\n\tv_accvgpr_write_b32 a250, v18\n\tv_accvgpr_write_b32 a251, v19"); FLAGSV(1, 0);
      SREAD(); REQV(80, 1, 8, 0); VPROC(96); HEAR();
      SREAD(); REQV(96, 1, 8, 0); VPROC(112); PACKV(3, "v_mov_b32 v136, v8\n\tv_mov_b32 v137, v9\n\tv_mov_b32 v138, v10\n\tv_mov_b32 v139, v11\n\tv_mov_b32 v140, v12\n\tv_mov_b32 v141, v13\n\tv_mov_b32 v142, v14\n\tv_mov_b32 v143, v15\n\tv_mov_b32 v144, v16\n\tv_mov_b32 v145, v17\n\tv_mov_b32 v146, v18\n\tv_mov_b32 v147, v19");
      SREAD(); REQV(112, 1, 8, 0); VPROC(64);
      SREAD(); REQV(64, 1, 8, 1); VPROC(80); PACKV(1, "v_mov_b32 v148, v8\n\tv_mov_b32 v149, v9\n\tv_mov_b32 v150, v10\n\tv_mov_b32 v151, v11\n\tv_mov_b32 v152, v12\n\tv_mov_b32 v153, v13\n\tv_mov_b32 v154, v14\n\tv_mov_b32 v155, v15\n\tv_mov_b32 v156, v16\n\tv_mov_b32 v157, v17\n\tv_mov_b32 v158, v18\n\tv_mov_b32 v159, v19");
      SREAD(); REQV(80, 1, 8, 0); VPROC(96); HEAR();
      SREAD(); REQV(96, 1, 8, 0); VPROC(112); PACKV(3, "v_mov_b32 v160, v8\n\tv_mov_b32 v161, v9\n\tv_mov_b32 v162, v10\n\tv_mov_b32 v163, v11\n\tv_mov_b32 v164, v12\n\tv_mov_b32 v165, v13\n\tv_mov_b32 v166, v14\n\tv_mov_b32 v167, v15\n\tv_mov_b32 v168, v16\n\tv_mov_b32 v169, v17\n\tv_mov_b32 v170, v18\n\tv_mov_b32 v171, v19");
      SREAD(); REQV(112, 1, 8, 0); VPROC(64);
      SREAD(); REQV(64, 1, 8, 1); VPROC(80); PACKV(1, "v_mov_b32 v172, v8\n\tv_mov_b32 v173, v9\n\tv_mov_b32 v174, v10\n\tv_mov_b32 v175, v11\n\tv_mov_b32 v176, v12\n\tv_mov_b32 v177, v13\n\tv_mov_b32 v178, v14\n\tv_mov_b32 v179, v15\n\tv_mov_b32 v180, v16\n\tv_mov_b32 v181, v17\n\tv_mov_b32 v182, v18\n\tv_mov_b32 v183, v19"); FLAGSV(2, 0);
      SREAD(); REQV(80, 1, 8, 0); VPROC(96); HEAR();
      SREAD(); REQV(96, 1, 8, 0); VPROC(112); PACKV(3, "v_mov_b32 v184, v8\n\tv_mov_b32 v185, v9\n\tv_mov_b32 v186, v10\n\tv_mov_b32 v187, v11\n\tv_mov_b32 v188, v12\n\tv_mov_b32 v189, v13\n\tv_mov_b32 v190, v14\n\tv_mov_b32 v191, v15\n\tv_mov_b32 v192, v16\n\tv_mov_b32 v193, v17\n\tv_mov_b32 v194, v18\n\tv_mov_b32 v195, v19");
      SREAD(); REQV(112, 1, 8, 0); VPROC(64);
      SREAD(); REQV(64, 1, 8, 1); VPROC(80); PACKV(1, "v_mov_b32 v196, v8\n\tv_mov_b32 v197, v9\n\tv_mov_b32 v198, v10\n\tv_mov_b32 v199, v11\n\tv_mov_b32 v200, v12\n\tv_mov_b32 v201, v13\n\tv_mov_b32 v202, v14\n\tv_mov_b32 v203, v15\n\tv_mov_b32 v204, v16\n\tv_mov_b32 v205, v17\n\tv_mov_b32 v206, v18\n\tv_mov_b32 v207, v19");
      SREAD(); REQV(80, 1, 8, 0); VPROC(96); HEAR();
      SREAD(); REQV(96, 1, 8, 0); VPROC(112); PACKV(3, "v_mov_b32 v208, v8\n\tv_mov_b32 v209, v9\n\tv_mov_b32 v210, v10\n\tv_mov_b32 v211, v11\n\tv_mov_b32 v212, v12\n\tv_mov_b32 v213, v13\n\tv_mov_b32 v214, v14\n\tv_mov_b32 v215, v15\n\tv_mov_b32 v216, v16\n\tv_mov_b32 v217, v17\n\tv_mov_b32 v218, v18\n\tv_mov_b32 v219, v19");
      SREAD(); REQV(112, 1, 8, 0); VPROC(64);
      SREAD(); REQV(64, 1, 8, 1); VPROC(80); PACKV(1, "v_mov_b32 v220, v8\n\tv_mov_b32 v221, v9\n\tv_mov_b32 v222, v10\n\tv_mov_b32 v223, v11\n\tv_mov_b32 v224, v12\n\tv_mov_b32 v225, v13\n\tv_mov_b32 v226, v14\n\tv_mov_b32 v227, v15\n\tv_mov_b32 v228, v16\n\tv_mov_b32 v229, v17\n\tv_mov_b32 v230, v18\n\tv_mov_b32 v231, v19"); FLAGSV(3, 0);
      SREAD(); REQV(80, 1, 8, 0); VPROC(96); HEAR();
      SREAD(); REQV(96, 1, 8, 0); VPROC(112); PACKV(3, "v_mov_b32 v232, v8\n\tv_mov_b32 v233, v9\n\tv_mov_b32 v234, v10\n\tv_mov_b32 v235, v11\n\tv_mov_b32 v236, v12\n\tv_mov_b32 v237, v13\n\tv_mov_b32 v238, v14\n\tv_mov_b32 v239, v15\n\tv_mov_b32 v240, v16\n\tv_mov_b32 v241, v17\n\tv_mov_b32 v242, v18\n\tv_mov_b32 v243, v19");
      SREAD(); REQV(112, 1, 8, 0); VPROC(64);
      SREAD(); REQV(64, 1, 8, 1); VPROC(80); PACKV(1, "v_mov_b32 v244, v8\n\tv_mov_b32 v245, v9\n\tv_mov_b32 v246, v10\n\tv_mov_b32 v247, v11\n\tv_mov_b32 v248, v12\n\tv_mov_b32 v249, v13\n\tv_mov_b32 v250, v14\n\tv_mov_b32 v251, v15\n\tv_mov_b32 v252, v16\n\tv_mov_b32 v253, v17\n\tv_mov_b32 v254, v18\n\tv_mov_b32 v255, v19");
      SREAD(); REQV(80, 1, 8, 0); VPROC(96); HEAR();
      SREAD(); REQV(96, 1, 8, 0); VPROC(112); PACKV(3, "v_mov_b32 v128, v8\n\tv_mov_b32 v129, v9\n\tv_mov_b32 v130, v10\n\tv_mov_b32 v131, v11\n\tv_mov_b32 v132, v12\n\tv_mov_b32 v133, v13\n\tv_mov_b32 v134, v14\n\tv_mov_b32 v135, v15\n\tv_accvgpr_write_b32 a252, v16\n\tv_accvgpr_write_b32 a253, v17\n\tv_accvgpr_write_b32 a254, v18\n\tv_accvgpr_write_b32 a255, v19"); FLAGSV(4, 8);
      if (!isP) {
        SREAD(); REQV(112, 1, 8, 0); VPROC(64);
        SREAD(); REQV(64, 1, 8, 1); VPROC(80);
        SREAD(); REQV(80, 1, 8, 0); VPROC(96);
        SREAD(); REQV(96, 1, 8, 0); VPROC(112);
      }
      SREAD(); REQV(112, 1, 8, 0); VPROC(64);
      SREAD(); REQV(64, 1, 8, 1); VPROC(80);
      SCORES(LDS_CMB + w * 1024); SREAD(); REQV(80, 1, 8, 0); VPROC(96);
      SREAD(); REQV(96, 1, 8, 0); VPROC(112);
      SREAD(); REQV(112, 1, 8, 0); VPROC(64);
      SREAD(); REQV(64, 1, 8, 1); VPROC(80);
      SREAD(); REQV(80, 1, 8, 0); VPROC(96);
      wg_barrier();
      if (isP) { load_rows(false); vmwait<26>(); SREAD(); VPROC(112); vmwait<22>(); SREAD(); VPROC(64); vmwait<18>(); SREAD(); VPROC(80); }
      else { vmwait<8>(); load_gu(0, EARLY); SREAD(); VPROC(112); vmwait<4 + EARLY>(); load_gu(EARLY, EARLY); SREAD(); VPROC(64); vmwait<2 * EARLY>(); SREAD(); VPROC(80); }
    } else {
      att_setup();
      v16u buf;
      kv.sok = opqs(((isP ? NPD : NFD) - 5) * 16384);
      buf_load(kv, buf);
      v4u t0, t1;
      q_issue(t0, t1);
      { const v2u b = *LDSP(v2u, LDS_BASE + opqs(w0) * 8); kv.bk = __builtin_amdgcn_readfirstlane(b.x); kv.bv = __builtin_amdgcn_readfirstlane(b.y); }
      pin_setup(kv);
      asm volatile("s_waitcnt vmcnt(0)" : "+v"(t0), "+v"(t1)::"memory");
      q_finish(t0, t1);
      pace_start(1, now16());
      kv.sok = opqs(0);
      { PROCB(8, 96); PROCB(16, 104); LOADK(96, 1); }
      { BUFK(2, 4096); PROCB(8, 112); PROCB(16, 120); LOADK(112, 1); }
      { BUFK(2, 8192); PROCB(8, 64); PROCB(16, 72); LOADK(64, 1); }
      { BUFK(2, 12288); PROCB(8, 80); PROCB(16, 88);  }
      SCORES(LDS_WIN + w * (NW * 4096) + 16384); BUFK(2, 16384); KPROCB(0, 128); BUFK(2, 0); KPROCB(0, 128);
      const unsigned pool = LDS_WIN + opqs(w0) * (NW * 4096);
      exc_load_scan<0>(kv); packed_scan(kv, pool + 0, 229376); REQK(80, 1, 8, 0); KPROC(96, 0, 128);
      packed_scan(kv, pool + 4096, 231424); REQK(96, 1, 8, 1); KPROC(112, 0, 128); HEAR();
      REQK(112, 1, 8, 0); KPROC(64, 0, 128);
      packed_scan(kv, pool + 8192, 262144); REQK(64, 1, 8, 0); KPROC(80, 0, 128);
      packed_scan(kv, pool + 12288, 264192); REQK(80, 1, 8, 0); KPROC(96, 0, 128); exc_load<0>(kv); PPROC(0, "v_accvgpr_read_b32 v8, a0\n\tv_accvgpr_read_b32 v9, a1\n\tv_accvgpr_read_b32 v10, a2\n\tv_accvgpr_read_b32 v11, a3\n\tv_accvgpr_read_b32 v12, a4\n\tv_accvgpr_read_b32 v13, a5\n\tv_accvgpr_read_b32 v14, a156\n\tv_accvgpr_read_b32 v15, a157\n\tv_accvgpr_read_b32 v16, a158\n\tv_accvgpr_read_b32 v17, a159\n\tv_accvgpr_read_b32 v18, a160\n\tv_accvgpr_read_b32 v19, a161");
      REQK(96, 1, 8, 1); KPROC(112, 0, 128);
      packed_scan(kv, pool + 0, 294912); REQK(112, 1, 8, 0); KPROC(64, 0, 128); PPROC(4096, "v_accvgpr_read_b32 v8, a6\n\tv_accvgpr_read_b32 v9, a7\n\tv_accvgpr_read_b32 v10, a8\n\tv_accvgpr_read_b32 v11, a9\n\tv_accvgpr_read_b32 v12, a10\n\tv_accvgpr_read_b32 v13, a11\n\tv_accvgpr_read_b32 v14, a162\n\tv_accvgpr_read_b32 v15, a163\n\tv_accvgpr_read_b32 v16, a164\n\tv_accvgpr_read_b32 v17, a165\n\tv_accvgpr_read_b32 v18, a166\n\tv_accvgpr_read_b32 v19, a167"); HEAR();
      REQK(64, 1, 8, 0); KPROC(80, 0, 128);
      packed_scan(kv, pool + 4096, 296960); REQK(80, 1, 8, 0); KPROC(96, 0, 128); PPROC(8192, "v_accvgpr_read_b32 v8, a12\n\tv_accvgpr_read_b32 v9, a13\n\tv_accvgpr_read_b32 v10, a14\n\tv_accvgpr_read_b32 v11, a15\n\tv_accvgpr_read_b32 v12, a16\n\tv_accvgpr_read_b32 v13, a17\n\tv_accvgpr_read_b32 v14, a168\n\tv_accvgpr_read_b32 v15, a169\n\tv_accvgpr_read_b32 v16, a170\n\tv_accvgpr_read_b32 v17, a171\n\tv_accvgpr_read_b32 v18, a172\n\tv_accvgpr_read_b32 v19, a173");
      packed_scan(kv, pool + 8192, 327680); REQK(96, 1, 8, 1); KPROC(112, 0, 128); PPROC(12288, "v_accvgpr_read_b32 v8, a18\n\tv_accvgpr_read_b32 v9, a19\n\tv_accvgpr_read_b32 v10, a20\n\tv_accvgpr_read_b32 v11, a21\n\tv_accvgpr_read_b32 v12, a22\n\tv_accvgpr_read_b32 v13, a23\n\tv_accvgpr_read_b32 v14, a174\n\tv_accvgpr_read_b32 v15, a175\n\tv_accvgpr_read_b32 v16, a176\n\tv_accvgpr_read_b32 v17, a177\n\tv_accvgpr_read_b32 v18, a178\n\tv_accvgpr_read_b32 v19, a179");
      REQK(112, 2, 8, 0); KPROC(64, 0, 128);
      packed_scan(kv, pool + 12288, 329728); REQK(64, 2, 8, 0); KPROC(80, 0, 128); PPROC(0, "v_accvgpr_read_b32 v8, a24\n\tv_accvgpr_read_b32 v9, a25\n\tv_accvgpr_read_b32 v10, a26\n\tv_accvgpr_read_b32 v11, a27\n\tv_accvgpr_read_b32 v12, a28\n\tv_accvgpr_read_b32 v13, a29\n\tv_accvgpr_read_b32 v14, a180\n\tv_accvgpr_read_b32 v15, a181\n\tv_accvgpr_read_b32 v16, a182\n\tv_accvgpr_read_b32 v17, a183\n\tv_accvgpr_read_b32 v18, a184\n\tv_accvgpr_read_b32 v19, a185"); HEAR();
      REQK(80, 2, 8, 0); KPROC(96, 0, 128);
      exc_load_scan<1>(kv); packed_scan(kv, pool + 0, 360448); REQK(96, 2, 8, 1); KPROC(112, 0, 128); PPROC(4096, "v_accvgpr_read_b32 v8, a30\n\tv_accvgpr_read_b32 v9, a31\n\tv_accvgpr_read_b32 v10, a32\n\tv_accvgpr_read_b32 v11, a33\n\tv_accvgpr_read_b32 v12, a34\n\tv_accvgpr_read_b32 v13, a35\n\tv_accvgpr_read_b32 v14, a186\n\tv_accvgpr_read_b32 v15, a187\n\tv_accvgpr_read_b32 v16, a188\n\tv_accvgpr_read_b32 v17, a189\n\tv_accvgpr_read_b32 v18, a190\n\tv_accvgpr_read_b32 v19, a191");
      packed_scan(kv, pool + 4096, 362496); REQK(112, 2, 8, 0); KPROC(64, 0, 128); PPROC(8192, "v_accvgpr_read_b32 v8, a36\n\tv_accvgpr_read_b32 v9, a37\n\tv_accvgpr_read_b32 v10, a38\n\tv_accvgpr_read_b32 v11, a39\n\tv_accvgpr_read_b32 v12, a40\n\tv_accvgpr_read_b32 v13, a41\n\tv_accvgpr_read_b32 v14, a192\n\tv_accvgpr_read_b32 v15, a193\n\tv_accvgpr_read_b32 v16, a194\n\tv_accvgpr_read_b32 v17, a195\n\tv_accvgpr_read_b32 v18, a196\n\tv_accvgpr_read_b32 v19, a197");
      REQK(64, 2, 8, 0); KPROC(80, 0, 128);
      if (!isP) {
        REQK(80, 31, 8, 0); KPROC(96, 0, 128); HEAR();
        REQK(96, 1, 8, 1); KPROC(112, 0, 128);
        REQK(112, 1, 8, 0); KPROC(64, 0, 128);
        REQK(64, 1, 8, 0); KPROC(80, 0, 128);
        kv.sok -= 34 * 16384;
      }
      packed_scan(kv, pool + 8192, 393216); REQK(80, 2, 8, 0); KPROC(96, 0, 128); PPROC(12288, "v_accvgpr_read_b32 v8, a42\n\tv_accvgpr_read_b32 v9, a43\n\tv_accvgpr_read_b32 v10, a44\n\tv_accvgpr_read_b32 v11, a45\n\tv_accvgpr_read_b32 v12, a46\n\tv_accvgpr_read_b32 v13, a47\n\tv_accvgpr_read_b32 v14, a198\n\tv_accvgpr_read_b32 v15, a199\n\tv_accvgpr_read_b32 v16, a200\n\tv_accvgpr_read_b32 v17, a201\n\tv_accvgpr_read_b32 v18, a202\n\tv_accvgpr_read_b32 v19, a203");
      REQK(96, 2, 8, 1); KPROC(112, 0, 128); HEAR();
      packed_scan(kv, pool + 12288, 395264); REQK(112, 2, 8, 0); KPROC(64, 0, 128); exc_load<1>(kv); PPROC(0, "v_accvgpr_read_b32 v8, a48\n\tv_accvgpr_read_b32 v9, a49\n\tv_accvgpr_read_b32 v10, a50\n\tv_accvgpr_read_b32 v11, a51\n\tv_accvgpr_read_b32 v12, a52\n\tv_accvgpr_read_b32 v13, a53\n\tv_accvgpr_read_b32 v14, a204\n\tv_accvgpr_read_b32 v15, a205\n\tv_accvgpr_read_b32 v16, a206\n\tv_accvgpr_read_b32 v17, a207\n\tv_accvgpr_read_b32 v18, a208\n\tv_accvgpr_read_b32 v19, a209");
      packed_scan(kv, pool + 0, 425984); REQK(64, 2, 8, 0); KPROC(80, 0, 128); PPROC(4096, "v_accvgpr_read_b32 v8, a54\n\tv_accvgpr_read_b32 v9, a55\n\tv_accvgpr_read_b32 v10, a56\n\tv_accvgpr_read_b32 v11, a57\n\tv_accvgpr_read_b32 v12, a58\n\tv_accvgpr_read_b32 v13, a59\n\tv_accvgpr_read_b32 v14, a210\n\tv_accvgpr_read_b32 v15, a211\n\tv_accvgpr_read_b32 v16, a212\n\tv_accvgpr_read_b32 v17, a213\n\tv_accvgpr_read_b32 v18, a214\n\tv_accvgpr_read_b32 v19, a215");
      REQK(80, 2, 8, 0); KPROC(96, 0, 128);
      packed_scan(kv, pool + 4096, 428032); REQK(96, 2, 8, 1); SCORES(LDS_RET + w * (NL * 4096) + 15360); KPROC(112, 0, 128); PPROC(8192, "v_accvgpr_read_b32 v8, a60\n\tv_accvgpr_read_b32 v9, a61\n\tv_accvgpr_read_b32 v10, a62\n\tv_accvgpr_read_b32 v11, a63\n\tv_accvgpr_read_b32 v12, a64\n\tv_accvgpr_read_b32 v13, a65\n\tv_accvgpr_read_b32 v14, a216\n\tv_accvgpr_read_b32 v15, a217\n\tv_accvgpr_read_b32 v16, a218\n\tv_accvgpr_read_b32 v17, a219\n\tv_accvgpr_read_b32 v18, a220\n\tv_accvgpr_read_b32 v19, a221");
      REQK(112, 2, 8, 0); KPROC(64, 0, 128); HEAR();
      packed_scan(kv, pool + 8192, 458752); REQK(64, 2, 8, 0); KPROC(80, 0, 128); PPROC(12288, "v_accvgpr_read_b32 v8, a66\n\tv_accvgpr_read_b32 v9, a67\n\tv_accvgpr_read_b32 v10, a68\n\tv_accvgpr_read_b32 v11, a69\n\tv_accvgpr_read_b32 v12, a70\n\tv_accvgpr_read_b32 v13, a71\n\tv_accvgpr_read_b32 v14, a222\n\tv_accvgpr_read_b32 v15, a223\n\tv_accvgpr_read_b32 v16, a224\n\tv_accvgpr_read_b32 v17, a225\n\tv_accvgpr_read_b32 v18, a226\n\tv_accvgpr_read_b32 v19, a227");
      packed_scan(kv, pool + 12288, 460800); REQK(80, 2, 8, 0); KPROC(96, 0, 128); PPROC(0, "v_accvgpr_read_b32 v8, a72\n\tv_accvgpr_read_b32 v9, a73\n\tv_accvgpr_read_b32 v10, a74\n\tv_accvgpr_read_b32 v11, a75\n\tv_accvgpr_read_b32 v12, a76\n\tv_accvgpr_read_b32 v13, a77\n\tv_accvgpr_read_b32 v14, a228\n\tv_accvgpr_read_b32 v15, a229\n\tv_accvgpr_read_b32 v16, a230\n\tv_accvgpr_read_b32 v17, a231\n\tv_accvgpr_read_b32 v18, a232\n\tv_accvgpr_read_b32 v19, a233");
      REQK(96, 2, 8, 1); KPROC(112, 0, 128);
      exc_load_scan<2>(kv); packed_scan(kv, pool + 0, 491520); REQK(112, 2, 8, 0); KPROC(64, 0, 128); PPROC(4096, "v_accvgpr_read_b32 v8, a78\n\tv_accvgpr_read_b32 v9, a79\n\tv_accvgpr_read_b32 v10, a80\n\tv_accvgpr_read_b32 v11, a81\n\tv_accvgpr_read_b32 v12, a82\n\tv_accvgpr_read_b32 v13, a83\n\tv_accvgpr_read_b32 v14, a234\n\tv_accvgpr_read_b32 v15, a235\n\tv_accvgpr_read_b32 v16, a236\n\tv_accvgpr_read_b32 v17, a237\n\tv_accvgpr_read_b32 v18, a238\n\tv_accvgpr_read_b32 v19, a239");
      REQK(64, 2, 8, 0); KPROC(80, 0, 128); HEAR();
      packed_scan(kv, pool + 4096, 493568); REQK(80, 1, 8, 0); KPROC(96, 0, 128); PPROC(8192, "v_accvgpr_read_b32 v8, a84\n\tv_accvgpr_read_b32 v9, a85\n\tv_accvgpr_read_b32 v10, a86\n\tv_accvgpr_read_b32 v11, a87\n\tv_accvgpr_read_b32 v12, a88\n\tv_accvgpr_read_b32 v13, a89\n\tv_accvgpr_read_b32 v14, a240\n\tv_accvgpr_read_b32 v15, a241\n\tv_accvgpr_read_b32 v16, a242\n\tv_accvgpr_read_b32 v17, a243\n\tv_accvgpr_read_b32 v18, a244\n\tv_accvgpr_read_b32 v19, a245");
      packed_scan(kv, pool + 8192, 524288); REQK(96, 1, 8, 1); SCORES(LDS_CMB + w * 1024); KPROC(112, 0, 128); PPROC(12288, "v_accvgpr_read_b32 v8, a90\n\tv_accvgpr_read_b32 v9, a91\n\tv_accvgpr_read_b32 v10, a92\n\tv_accvgpr_read_b32 v11, a93\n\tv_accvgpr_read_b32 v12, a94\n\tv_accvgpr_read_b32 v13, a95\n\tv_accvgpr_read_b32 v14, a246\n\tv_accvgpr_read_b32 v15, a247\n\tv_accvgpr_read_b32 v16, a248\n\tv_accvgpr_read_b32 v17, a249\n\tv_accvgpr_read_b32 v18, a250\n\tv_accvgpr_read_b32 v19, a251");
      REQK(112, 1, 8, 0); KPROC(64, 0, 128);
      packed_scan(kv, pool + 12288, 526336); REQK(64, 1, 8, 0); KPROC(80, 0, 128); exc_load<2>(kv); PPROC(0, "v_accvgpr_read_b32 v8, a96\n\tv_accvgpr_read_b32 v9, a97\n\tv_accvgpr_read_b32 v10, a98\n\tv_accvgpr_read_b32 v11, a99\n\tv_accvgpr_read_b32 v12, a100\n\tv_accvgpr_read_b32 v13, a101\n\tv_mov_b32 v14, v136\n\tv_mov_b32 v15, v137\n\tv_mov_b32 v16, v138\n\tv_mov_b32 v17, v139\n\tv_mov_b32 v18, v140\n\tv_mov_b32 v19, v141");
      REQK(80, 1, 8, 0); KPROC(96, 0, 128); HEAR();
      packed_scan(kv, pool + 0, 557056); REQK(96, 1, 8, 1); KPROC(112, 0, 128); PPROC(4096, "v_accvgpr_read_b32 v8, a102\n\tv_accvgpr_read_b32 v9, a103\n\tv_accvgpr_read_b32 v10, a104\n\tv_accvgpr_read_b32 v11, a105\n\tv_accvgpr_read_b32 v12, a106\n\tv_accvgpr_read_b32 v13, a107\n\tv_mov_b32 v14, v142\n\tv_mov_b32 v15, v143\n\tv_mov_b32 v16, v144\n\tv_mov_b32 v17, v145\n\tv_mov_b32 v18, v146\n\tv_mov_b32 v19, v147");
      packed_scan(kv, pool + 4096, 559104); kv.sov = opqs(((isP ? NPD : NFD) - 6) * 16384); REQV(112, 1, 8, 0); kv.sov = opqs(0u - 16384u); KPROC(64, 0, 128); PPROC(8192, "v_accvgpr_read_b32 v8, a108\n\tv_accvgpr_read_b32 v9, a109\n\tv_accvgpr_read_b32 v10, a110\n\tv_accvgpr_read_b32 v11, a111\n\tv_accvgpr_read_b32 v12, a112\n\tv_accvgpr_read_b32 v13, a113\n\tv_mov_b32 v14, v148\n\tv_mov_b32 v15, v149\n\tv_mov_b32 v16, v150\n\tv_mov_b32 v17, v151\n\tv_mov_b32 v18, v152\n\tv_mov_b32 v19, v153");
      REQV(64, 1, 8, 0); KPROC(80, 0, 128);
      packed_scan(kv, pool + 8192, 589824); REQV(80, 1, 8, 0); KPROC(96, 0, 128); PPROC(12288, "v_accvgpr_read_b32 v8, a114\n\tv_accvgpr_read_b32 v9, a115\n\tv_accvgpr_read_b32 v10, a116\n\tv_accvgpr_read_b32 v11, a117\n\tv_accvgpr_read_b32 v12, a118\n\tv_accvgpr_read_b32 v13, a119\n\tv_mov_b32 v14, v154\n\tv_mov_b32 v15, v155\n\tv_mov_b32 v16, v156\n\tv_mov_b32 v17, v157\n\tv_mov_b32 v18, v158\n\tv_mov_b32 v19, v159");
      SCORES(LDS_WIN + w * (NW * 4096) + 16384); SREAD(); REQV(96, 1, 8, 1); VPROC(112); HEAR();
      packed_scan(kv, pool + 12288, 591872); SREAD(); REQV(112, 1, 8, 0); VPROC(64); PPROC(0, "v_accvgpr_read_b32 v8, a120\n\tv_accvgpr_read_b32 v9, a121\n\tv_accvgpr_read_b32 v10, a122\n\tv_accvgpr_read_b32 v11, a123\n\tv_accvgpr_read_b32 v12, a124\n\tv_accvgpr_read_b32 v13, a125\n\tv_mov_b32 v14, v160\n\tv_mov_b32 v15, v161\n\tv_mov_b32 v16, v162\n\tv_mov_b32 v17, v163\n\tv_mov_b32 v18, v164\n\tv_mov_b32 v19, v165");
      SREAD(); REQV(64, 1, 8, 0); VPROC(80);
      exc_load_scan<3>(kv); packed_scan(kv, pool + 0, 622592); SREAD(); REQV(80, 1, 8, 0); VPROC(96); PPROC(4096, "v_accvgpr_read_b32 v8, a126\n\tv_accvgpr_read_b32 v9, a127\n\tv_accvgpr_read_b32 v10, a128\n\tv_accvgpr_read_b32 v11, a129\n\tv_accvgpr_read_b32 v12, a130\n\tv_accvgpr_read_b32 v13, a131\n\tv_mov_b32 v14, v166\n\tv_mov_b32 v15, v167\n\tv_mov_b32 v16, v168\n\tv_mov_b32 v17, v169\n\tv_mov_b32 v18, v170\n\tv_mov_b32 v19, v171");
      SREAD(); REQV(96, 1, 8, 1); VPROC(112);
      packed_scan(kv, pool + 4096, 624640); SREAD(); REQV(112, 1, 8, 0); VPROC(64); PPROC(8192, "v_accvgpr_read_b32 v8, a132\n\tv_accvgpr_read_b32 v9, a133\n\tv_accvgpr_read_b32 v10, a134\n\tv_accvgpr_read_b32 v11, a135\n\tv_accvgpr_read_b32 v12, a136\n\tv_accvgpr_read_b32 v13, a137\n\tv_mov_b32 v14, v172\n\tv_mov_b32 v15, v173\n\tv_mov_b32 v16, v174\n\tv_mov_b32 v17, v175\n\tv_mov_b32 v18, v176\n\tv_mov_b32 v19, v177"); HEAR();
      SREAD(); REQV(64, 1, 8, 0); VPROC(80);
      packed_scan(kv, pool + 8192, 655360); SREAD(); REQV(80, 1, 8, 0); VPROC(96); PPROC(12288, "v_accvgpr_read_b32 v8, a138\n\tv_accvgpr_read_b32 v9, a139\n\tv_accvgpr_read_b32 v10, a140\n\tv_accvgpr_read_b32 v11, a141\n\tv_accvgpr_read_b32 v12, a142\n\tv_accvgpr_read_b32 v13, a143\n\tv_mov_b32 v14, v178\n\tv_mov_b32 v15, v179\n\tv_mov_b32 v16, v180\n\tv_mov_b32 v17, v181\n\tv_mov_b32 v18, v182\n\tv_mov_b32 v19, v183");
      SREAD(); REQV(96, 1, 8, 1); VPROC(112);
      packed_scan(kv, pool + 12288, 657408); SREAD(); REQV(112, 1, 8, 0); VPROC(64); exc_load<3>(kv); PPROC(0, "v_accvgpr_read_b32 v8, a144\n\tv_accvgpr_read_b32 v9, a145\n\tv_accvgpr_read_b32 v10, a146\n\tv_accvgpr_read_b32 v11, a147\n\tv_accvgpr_read_b32 v12, a148\n\tv_accvgpr_read_b32 v13, a149\n\tv_mov_b32 v14, v184\n\tv_mov_b32 v15, v185\n\tv_mov_b32 v16, v186\n\tv_mov_b32 v17, v187\n\tv_mov_b32 v18, v188\n\tv_mov_b32 v19, v189");
      SREAD(); REQV(64, 1, 8, 0); VPROC(80); HEAR();
      packed_scan(kv, pool + 0, 688128); SREAD(); REQV(80, 1, 8, 0); VPROC(96); PPROC(4096, "v_accvgpr_read_b32 v8, a150\n\tv_accvgpr_read_b32 v9, a151\n\tv_accvgpr_read_b32 v10, a152\n\tv_accvgpr_read_b32 v11, a153\n\tv_accvgpr_read_b32 v12, a154\n\tv_accvgpr_read_b32 v13, a155\n\tv_mov_b32 v14, v190\n\tv_mov_b32 v15, v191\n\tv_mov_b32 v16, v192\n\tv_mov_b32 v17, v193\n\tv_mov_b32 v18, v194\n\tv_mov_b32 v19, v195");
      SREAD(); REQV(96, 2, 8, 1); VPROC(112);
      packed_scan(kv, pool + 4096, 690176); SREAD(); REQV(112, 2, 8, 0); VPROC(64); PPROC(8192, "ds_read_b128 v[20:23], v1 offset:0\n\tds_read_b128 v[24:27], v1 offset:1024\n\ts_waitcnt lgkmcnt(0)\n\tv_mov_b32 v8, v20\n\tv_mov_b32 v9, v21\n\tv_mov_b32 v10, v22\n\tv_mov_b32 v11, v23\n\tv_mov_b32 v12, v24\n\tv_mov_b32 v13, v25\n\tv_mov_b32 v14, v196\n\tv_mov_b32 v15, v197\n\tv_mov_b32 v16, v198\n\tv_mov_b32 v17, v199\n\tv_mov_b32 v18, v200\n\tv_mov_b32 v19, v201");
      SREAD(); REQV(64, 2, 8, 0); VPROC(80);
      packed_scan(kv, pool + 8192, 720896); SREAD(); REQV(80, 2, 8, 0); VPROC(96); PPROC(12288, "ds_read_b128 v[20:23], v1 offset:1024\n\tds_read_b128 v[24:27], v1 offset:2048\n\ts_waitcnt lgkmcnt(0)\n\tv_mov_b32 v8, v22\n\tv_mov_b32 v9, v23\n\tv_mov_b32 v10, v24\n\tv_mov_b32 v11, v25\n\tv_mov_b32 v12, v26\n\tv_mov_b32 v13, v27\n\tv_mov_b32 v14, v202\n\tv_mov_b32 v15, v203\n\tv_mov_b32 v16, v204\n\tv_mov_b32 v17, v205\n\tv_mov_b32 v18, v206\n\tv_mov_b32 v19, v207"); HEAR();
      SREAD(); REQV(96, 2, 8, 1); VPROC(112);
      packed_scan(kv, pool + 12288, 722944); SREAD(); REQV(112, 2, 8, 0); VPROC(64); PPROC(0, "ds_read_b128 v[20:23], v1 offset:3072\n\tds_read_b128 v[24:27], v1 offset:4096\n\ts_waitcnt lgkmcnt(0)\n\tv_mov_b32 v8, v20\n\tv_mov_b32 v9, v21\n\tv_mov_b32 v10, v22\n\tv_mov_b32 v11, v23\n\tv_mov_b32 v12, v24\n\tv_mov_b32 v13, v25\n\tv_mov_b32 v14, v208\n\tv_mov_b32 v15, v209\n\tv_mov_b32 v16, v210\n\tv_mov_b32 v17, v211\n\tv_mov_b32 v18, v212\n\tv_mov_b32 v19, v213");
      if (!isP) {
        SREAD(); REQV(64, 31, 8, 0); VPROC(80);
        SREAD(); REQV(80, 1, 8, 0); VPROC(96);
        SREAD(); REQV(96, 1, 8, 1); VPROC(112); HEAR();
        SREAD(); REQV(112, 1, 8, 0); VPROC(64);
        kv.sov -= 34 * 16384;
      }
      SREAD(); REQV(64, 2, 8, 0); VPROC(80);
      exc_load_scan<4>(kv); packed_scan(kv, pool + 0, 753664); SREAD(); REQV(80, 2, 8, 0); VPROC(96); PPROC(4096, "ds_read_b128 v[20:23], v1 offset:4096\n\tds_read_b128 v[24:27], v1 offset:5120\n\ts_waitcnt lgkmcnt(0)\n\tv_mov_b32 v8, v22\n\tv_mov_b32 v9, v23\n\tv_mov_b32 v10, v24\n\tv_mov_b32 v11, v25\n\tv_mov_b32 v12, v26\n\tv_mov_b32 v13, v27\n\tv_mov_b32 v14, v214\n\tv_mov_b32 v15, v215\n\tv_mov_b32 v16, v216\n\tv_mov_b32 v17, v217\n\tv_mov_b32 v18, v218\n\tv_mov_b32 v19, v219");
      SREAD(); REQV(96, 2, 8, 1); VPROC(112);
      packed_scan(kv, pool + 4096, 755712); SREAD(); REQV(112, 2, 8, 0); VPROC(64); PPROC(8192, "ds_read_b128 v[20:23], v1 offset:6144\n\tds_read_b128 v[24:27], v1 offset:7168\n\ts_waitcnt lgkmcnt(0)\n\tv_mov_b32 v8, v20\n\tv_mov_b32 v9, v21\n\tv_mov_b32 v10, v22\n\tv_mov_b32 v11, v23\n\tv_mov_b32 v12, v24\n\tv_mov_b32 v13, v25\n\tv_mov_b32 v14, v220\n\tv_mov_b32 v15, v221\n\tv_mov_b32 v16, v222\n\tv_mov_b32 v17, v223\n\tv_mov_b32 v18, v224\n\tv_mov_b32 v19, v225"); HEAR();
      SREAD(); REQV(64, 2, 8, 0); VPROC(80);
      packed_scan(kv, pool + 8192, 786432); SCORES(LDS_RET + w * (NL * 4096) + 15360); SREAD(); REQV(80, 2, 8, 0); VPROC(96); PPROC(12288, "ds_read_b128 v[20:23], v1 offset:7168\n\tds_read_b128 v[24:27], v1 offset:8192\n\ts_waitcnt lgkmcnt(0)\n\tv_mov_b32 v8, v22\n\tv_mov_b32 v9, v23\n\tv_mov_b32 v10, v24\n\tv_mov_b32 v11, v25\n\tv_mov_b32 v12, v26\n\tv_mov_b32 v13, v27\n\tv_mov_b32 v14, v226\n\tv_mov_b32 v15, v227\n\tv_mov_b32 v16, v228\n\tv_mov_b32 v17, v229\n\tv_mov_b32 v18, v230\n\tv_mov_b32 v19, v231");
      SREAD(); REQV(96, 2, 8, 1); VPROC(112);
      packed_scan(kv, pool + 12288, 788480); SREAD(); REQV(112, 2, 8, 0); VPROC(64); exc_load<4>(kv); PPROC(0, "ds_read_b128 v[20:23], v1 offset:9216\n\tds_read_b128 v[24:27], v1 offset:10240\n\ts_waitcnt lgkmcnt(0)\n\tv_mov_b32 v8, v20\n\tv_mov_b32 v9, v21\n\tv_mov_b32 v10, v22\n\tv_mov_b32 v11, v23\n\tv_mov_b32 v12, v24\n\tv_mov_b32 v13, v25\n\tv_mov_b32 v14, v232\n\tv_mov_b32 v15, v233\n\tv_mov_b32 v16, v234\n\tv_mov_b32 v17, v235\n\tv_mov_b32 v18, v236\n\tv_mov_b32 v19, v237");
      SREAD(); REQV(64, 2, 8, 0); VPROC(80);
      packed_scan(kv, pool + 0, 819200); SREAD(); REQV(80, 2, 8, 0); VPROC(96); PPROC(4096, "ds_read_b128 v[20:23], v1 offset:10240\n\tds_read_b128 v[24:27], v1 offset:11264\n\ts_waitcnt lgkmcnt(0)\n\tv_mov_b32 v8, v22\n\tv_mov_b32 v9, v23\n\tv_mov_b32 v10, v24\n\tv_mov_b32 v11, v25\n\tv_mov_b32 v12, v26\n\tv_mov_b32 v13, v27\n\tv_mov_b32 v14, v238\n\tv_mov_b32 v15, v239\n\tv_mov_b32 v16, v240\n\tv_mov_b32 v17, v241\n\tv_mov_b32 v18, v242\n\tv_mov_b32 v19, v243");
      SREAD(); REQV(96, 2, 8, 1); VPROC(112);
      packed_scan(kv, pool + 4096, 821248); SREAD(); REQV(112, 2, 8, 0); VPROC(64); PPROC(8192, "ds_read_b128 v[20:23], v1 offset:12288\n\tds_read_b128 v[24:27], v1 offset:13312\n\ts_waitcnt lgkmcnt(0)\n\tv_mov_b32 v8, v20\n\tv_mov_b32 v9, v21\n\tv_mov_b32 v10, v22\n\tv_mov_b32 v11, v23\n\tv_mov_b32 v12, v24\n\tv_mov_b32 v13, v25\n\tv_mov_b32 v14, v244\n\tv_mov_b32 v15, v245\n\tv_mov_b32 v16, v246\n\tv_mov_b32 v17, v247\n\tv_mov_b32 v18, v248\n\tv_mov_b32 v19, v249");
      SREAD(); REQV(64, 2, 8, 0); VPROC(80);
      SCORES(LDS_CMB + w * 1024); SREAD(); REQV(80, 1, 8, 0); VPROC(96); PPROC(12288, "ds_read_b128 v[20:23], v1 offset:13312\n\tds_read_b128 v[24:27], v1 offset:14336\n\ts_waitcnt lgkmcnt(0)\n\tv_mov_b32 v8, v22\n\tv_mov_b32 v9, v23\n\tv_mov_b32 v10, v24\n\tv_mov_b32 v11, v25\n\tv_mov_b32 v12, v26\n\tv_mov_b32 v13, v27\n\tv_mov_b32 v14, v250\n\tv_mov_b32 v15, v251\n\tv_mov_b32 v16, v252\n\tv_mov_b32 v17, v253\n\tv_mov_b32 v18, v254\n\tv_mov_b32 v19, v255");
      SREAD(); REQV(96, 1, 8, 1); VPROC(112); SREAD(); UVPROC(0, 62, "v_mov_b32 v14, v128\n\tv_mov_b32 v15, v129\n\tv_mov_b32 v16, v130\n\tv_mov_b32 v17, v131\n\tv_mov_b32 v18, v132\n\tv_mov_b32 v19, v133"); UVPROC(4096, 63, "v_mov_b32 v14, v134\n\tv_mov_b32 v15, v135\n\tv_accvgpr_read_b32 v16, a252\n\tv_accvgpr_read_b32 v17, a253\n\tv_accvgpr_read_b32 v18, a254\n\tv_accvgpr_read_b32 v19, a255");
      SREAD(); REQV(112, 1, 8, 0); VPROC(64);
      SREAD(); REQV(64, 1, 8, 0); VPROC(80);
      wg_barrier();
      if (isP) { load_rows(false); vmwait<26>(); SREAD(); VPROC(96); vmwait<22>(); SREAD(); VPROC(112); vmwait<18>(); SREAD(); VPROC(64); }
      else { vmwait<8>(); SREAD(); VPROC(96); vmwait<4>(); SREAD(); VPROC(112); vmwait<0>(); SREAD(); VPROC(64); }
    }

    {
      LANES;
      float l = kv.l + dppf<HMIR>(kv.l);
      l = xrow_sum(l);
      *LDSP(f32x4, LDS_CMB + w * 1024 + lane * 16) = kv.acc;
      if (lane == 0 || lane == 8) *LDSP(f32x2, LDS_CMB + 4096 + lane * 4 + w * 8) = f32x2{kv.m, l};
      wg_barrier();
      if (tid < 128) {
        const int hh = tid >> 6, e = tid & 63;
        const f32x4 ml0 = *LDSP(f32x4, LDS_CMB + 4096 + hh * 32), ml1 = *LDSP(f32x4, LDS_CMB + 4096 + hh * 32 + 16);
        f32x2 o[4];
#pragma unroll
        for (int i = 0; i < 4; ++i) o[i] = *LDSP(f32x2, LDS_CMB + i * 1024 + (16 * (e >> 4) + 8 * hh + (e & 7)) * 16 + ((e >> 3) & 1) * 8);
        const float M = fmaxf(fmaxf(ml0[0], ml0[2]), fmaxf(ml1[0], ml1[2]));
        const float f0 = __builtin_amdgcn_exp2f(ml0[0] - M), f1 = __builtin_amdgcn_exp2f(ml0[2] - M), f2 = __builtin_amdgcn_exp2f(ml1[0] - M), f3 = __builtin_amdgcn_exp2f(ml1[2] - M);
        const float o0 = (f0 * o[0].x + f1 * o[1].x) + (f2 * o[2].x + f3 * o[3].x), o1 = (f0 * o[0].y + f1 * o[1].y) + (f2 * o[2].y + f3 * o[3].y);
        const float L = (f0 * ml0[1] + f1 * ml0[3]) + (f2 * ml1[1] + f3 * ml1[3]);
        const unsigned pb = WS_PART + it * PART_BYTES + ((kvh * 16 + sp) * 2 + hh) * PART_H;
        put(pb + e * 16, __float_as_uint(o0), __float_as_uint(o1));
        if (e == 0) put(pb + 64 * 16, __float_as_uint(M), __float_as_uint(L));
      }
    }

    if (isP) {
      constexpr int PRE = 6;
      v4u wf[PRE];
      Proj pj;
      {
        LANES;
        vmwait<0>();
        wg_barrier();
        pj = proj_setup(I2, w, lane);
        sfor<PRE>([&](auto Nc) { wf[Nc.value] = proj_frag(I2, pj, Nc); });
        const int qd = pidx & 3;
        const unsigned pb = WS_PART + it * PART_BYTES + (((pidx >> 2) * 16 + (lane >> 2)) * 2 + (qd >> 1)) * PART_H;
        const unsigned po = pb + ((qd & 1) * 32 + w * 8 + (lane & 3) * 2) * 16;
        v4u t0 = get(po), t1 = get(po + 16), tm = get(pb + 64 * 16);
        for (unsigned spin = 0;; ++spin) {
          const bool ok = ok4(t0) && ok4(t1) && ok4(tm);
          if (__all(ok) || dead) break;
          if (spin > SPIN_CAP) { dead = true; break; }
          asm volatile("" ::: "memory");
          if (!ok) { t0 = get(po); t1 = get(po + 16); tm = get(pb + 64 * 16); }
        }
        SETTLED();
        const float ms = __uint_as_float(tm.x);
        float M = fmaxf(ms, dppf<ROR4>(ms)); M = fmaxf(M, dppf<ROR8>(M)); M = xrow_max(M);
        const float f = __builtin_amdgcn_exp2f(ms - M);
        float v[5] = {f * __uint_as_float(t0.x), f * __uint_as_float(t1.x), f * __uint_as_float(t0.z), f * __uint_as_float(t1.z), f * __uint_as_float(tm.z)};
#pragma unroll
        for (int i = 0; i < 5; ++i) { v[i] += dppf<ROR4>(v[i]); v[i] += dppf<ROR8>(v[i]); v[i] = xrow_sum(v[i]); }
        const float inv = __builtin_amdgcn_rcpf(v[4]);
        const int jj = lane & 3, slot = 4 * w + ((lane >> 2) & 1) * 2 + (jj >> 1);
        if (lane < 8) *LDSP(unsigned, LDS_XO + ((slot & ~7) + (slot & 3) * 2 + ((slot >> 2) & 1)) * 8 + (jj & 1) * 4) = lane < 4 ? cvtpk(v[0] * inv, v[1] * inv) : cvtpk(v[2] * inv, v[3] * inv);
      }
      wg_barrier();
      proj_run(I2, std::integral_constant<int, PRE>{}, pj, wf, WS_YO + it * YO_BYTES);
      LANES;
      if (ridx < 28) {
        const unsigned tile = ridx + 28 * (w >> 1);
        const unsigned xold = *LDSP(unsigned short, xh_addr(4 * (tile < 36 ? tile : 0) + (c16 >> 2)) + (c16 & 3) * 2);
        const v2u xo = reduce_cols(IC<4>{}, I2, WS_YO + it * YO_BYTES + tile * 8192 + (tid & 127) * 16, tile < 36, xold);
        if (tile < 36 && !(w & 1) && lane < 16 && !(lane & 3)) put(WS_X + (2 * it) * X_BYTES + tile * 128 + (lane >> 2) * 16, xo.x, xo.y);
        if (it == 0) load_wq(args_again()->wq);
      } else {
        wg_barrier();
        if (it == 0) load_wq(args_again()->wq);
        const unsigned tile = ridx - 28;
        unsigned char* const outp = args_again()->out;
        v4u t;
        for (unsigned spin = 0;; ++spin) {
          asm volatile("" ::: "memory");
          t = get(WS_X + (2 * it) * X_BYTES + tile * 128 + (c16 >> 2) * 16);
          if (__all(ok4(t)) || dead) break;
          if (spin > SPIN_CAP) { dead = true; break; }
        }
        SETTLED();
        const unsigned xw = (c16 & 2) ? t.z : t.x;
        const v2u xn = reduce_cols(IC<6>{}, IC<4>{}, WS_YD + it * YD_BYTES + tile * 24576 + tid * 16, true, (c16 & 1) ? xw >> 16 : xw);
        if (tid < 16 && !(tid & 3)) {
          if (it == 0) put(WS_X + X_BYTES + tile * 128 + (tid >> 2) * 16, xn.x, xn.y);
          else *(v2u*)(outp + (tile * 16 + tid) * 2) = xn;
        }
        if (it == 1 && tile == 0 && tid == 0) __builtin_amdgcn_raw_buffer_store_b32(opqs(tagA), rs, WS_EPOCH, 0, 16);
      }
    } else {
      load_gu(it ? 0 : 2 * EARLY, it ? 18 : 18 - 2 * EARLY);
      load_rows(true);
      vmwait<0>();
      wg_barrier();
      v4u wd[9];
      Proj pj;
      f32x4 d0 = {0, 0, 0, 0}, d1 = {0, 0, 0, 0}, d2 = {0, 0, 0, 0};
      Stat st;
      {
        LANES;
        const unsigned wa = LDS_WIN + (32 * w + 4 * g4 + (c16 >> 2)) * 96 + (lane & 3) * 8;
        v4u wf[9];
#pragma unroll
        for (int i = 0; i < 9; ++i) wf[i] = lds_frag(wa + (i / 3) * (128 * 96) + (i % 3) * 32, 16 * 96);
        const v2u xr = gather_x(WS_X + (2 * it) * X_BYTES, w, lane);
        stage_x(xr, w, lane);
        const unsigned xa = (c16 < 8 ? LDS_XH : LDS_ONES) + w * 64 + g4 * 16;
        v4u av[2];
#pragma unroll
        for (int i = 0; i < 2; ++i) av[i] = lds_r128(xa + i * 256);
        SB();
        mfma3(d0, d1, d2, av[0], wf[0], wf[1], wf[2]);
        av[0] = lds_r128(xa + 2 * 256);
#pragma unroll
        for (int i = 0; i < 3; ++i) wf[i] = lds_frag(wa + 3 * (128 * 96) + i * 32, 16 * 96);
        SB();
        mfma3(d0, d1, d2, av[1], wf[3], wf[4], wf[5]);
        av[1] = lds_r128(xa + 3 * 256);
        const unsigned k5 = w < 2 ? 4 : 3;
#pragma unroll
        for (int i = 0; i < 3; ++i) wf[3 + i] = lds_frag(wa + k5 * (128 * 96) + i * 32, 16 * 96);
        SB();
        mfma3(d0, d1, d2, av[0], wf[6], wf[7], wf[8]);
        av[0] = lds_r128(w < 2 ? xa + 4 * 256 : LDS_ZERO);
        stat_step(st, 0, lane_id());
        SB();
        mfma3(d0, d1, d2, av[1], wf[0], wf[1], wf[2]);
        stat_step(st, 1, lane);
        SB();
        mfma3(d0, d1, d2, av[0], wf[3], wf[4], wf[5]);
        stat_step(st, 2, lane);
        SB();
      }
      {
        LANES;
        mfma_fence(d0); mfma_fence(d1); mfma_fence(d2);
        *LDSP(float, LDS_RED + (((g4 >> 1) * 48 + (g4 & 1) * 16 + c16) * 4 + w) * 4) = (g4 & 1) ? d1[0] : d0[0];
        if (!(g4 & 1)) *LDSP(float, LDS_RED + (((g4 >> 1) * 48 + 32 + c16) * 4 + w) * 4) = d2[0];
        stat_step(st, 3, lane);
        stat_step(st, 4, lane);
        wg_barrier();
        const int l5 = lane & 31, col = (l5 < 24 ? l5 : 0) + (lane >> 5) * 24;
        const f32x4 ps = *LDSP(f32x4, LDS_RED + col * 16), pc = *LDSP(f32x4, LDS_RED + (48 + col) * 16);
        pj = proj_setup(I1, w, lane);
        sfor<9>([&](auto Nc) { wd[Nc.value] = proj_frag(I1, pj, Nc); });
        SB();
        const float mean = st.mean, inv = st.inv;
        const unsigned yb = cvtpk((((ps[0] + ps[1]) + (ps[2] + ps[3])) - mean * ((pc[0] + pc[1]) + (pc[2] + pc[3]))) * inv, 0.f) << 16;
        auto sw = __builtin_amdgcn_permlane32_swap(yb, yb, false, false);
        const float gb = __uint_as_float(sw[0]), ub = __uint_as_float(sw[1]);
        const float z2 = gb * (1.0f + 0.044715f * gb * gb) * (2.0f * 0.7978845608028654f * 1.4426950408889634f);
        const float th = 1.0f - 2.0f * __builtin_amdgcn_rcpf(__builtin_amdgcn_exp2f(z2) + 1.0f);
        const float ge = rbf(0.5f * gb * (1.0f + th));
        const unsigned hb = lane < 24 ? (cvtpk(ge * ub, 0.f) & 0xffffu) : 0u;
        const int q = lane >> 2;
        if (lane < 32) *LDSP(unsigned short, LDS_XO + w * 64 + (((q & 3) * 2 + (q >> 2)) * 4 + (lane & 3)) * 2) = (unsigned short)hb;
      }
      proj_run(I1, std::integral_constant<int, 9>{}, pj, wd, WS_YD + it * YD_BYTES);
      if (it == 0) { wg_barrier(); refill_window(); }
    }
  }
#undef LANES
}

}


extern "C" void kernel_launch(void* const* d_in, const int* in_sizes, int n_in, void* d_out, int out_size, void* d_ws, size_t ws_size,
                              hipStream_t stream) {
  Args a;
  a.x0 = (const unsigned char*)d_in[0];
  a.k = (const unsigned char*)d_in[1];
  a.v = (const unsigned char*)d_in[2];
  a.wq = (const unsigned char*)d_in[3];
  a.wo = (const unsigned char*)d_in[4];
  a.wg = (const unsigned char*)d_in[6];
  a.wu = (const unsigned char*)d_in[7];
  a.wd = (const unsigned char*)d_in[8];
  a.out = (unsigned char*)d_out;
  a.ws = (unsigned char*)d_ws;
  (void)hipFuncSetAttribute((const void*)fused_decode_kernel, hipFuncAttributeMaxDynamicSharedMemorySize, LDS_BYTES);
  hipLaunchKernelGGL(fused_decode_kernel, dim3(256), dim3(256), LDS_BYTES, stream, a);
}
```
